```python
import torch

def _softmax(x, axis=-1):
    return torch.softmax(x, dim=axis)
def _gelu(x):
    return torch.nn.functional.gelu(x, approximate="tanh")
def _silu(x):
    return torch.nn.functional.silu(x)
def _sigmoid(x):
    return torch.sigmoid(x)
def _layernorm(x, axis=-1, eps=1e-5):
    m = x.mean(dim=axis, keepdim=True)
    v = x.var(dim=axis, keepdim=True, unbiased=False)
    return (x - m) / torch.sqrt(v + eps)
def _scatter_add(tgt, idx, src, axis=0):
    return tgt.clone().index_add_(axis, idx, src)
def _segment_sum(src, seg, n):
    out = torch.zeros((n,) + src.shape[1:], dtype=src.dtype)
    return out.index_add_(0, seg, src)
def _pad(x, pw):
    flat = tuple(v for pair in reversed(pw) for v in pair)
    return torch.nn.functional.pad(x, flat)

kernel_name = 'concat_heads__ssm_conv_s4__spectral_gfnet__prompt'

def setup_inputs():

    _w = (lambda a: (lambda t: t.to(torch.bfloat16) if t.is_floating_point() else t)(torch.as_tensor(a)))
    def _A_setup():
        B, L, D, N = 1, 2048, 576, 128
        A_re = -(lambda _x: torch.exp(torch.as_tensor(_x)))((lambda *s: torch.randn(*s, dtype=torch.bfloat16) * 3.2)(D, N))
        A_im = torch.pi * (lambda *s: torch.randn(*s, dtype=torch.bfloat16) * 0.05)(D, N)
        B_re = (lambda *s: torch.randn(*s, dtype=torch.bfloat16) * 0.1)(D, N)
        B_im = (lambda *s: torch.randn(*s, dtype=torch.bfloat16) * 0.1)(D, N)
        C_re = (lambda *s: torch.randn(*s, dtype=torch.bfloat16) * 0.1)(D, N)
        C_im = (lambda *s: torch.randn(*s, dtype=torch.bfloat16) * 0.1)(D, N)
        log_dt = (lambda *s: torch.randn(*s, dtype=torch.bfloat16) * 0.05)(D) * 0.5 - 4.0
        Dskip = (lambda *s: torch.randn(*s, dtype=torch.bfloat16) * 0.05)(D)
        return {
            "u": _w((lambda *s: torch.randn(*s, dtype=torch.bfloat16) * 0.05)(B, L, D)),
            "A_re": _w(A_re),
            "A_im": _w(A_im),
            "B_re": _w(B_re),
            "B_im": _w(B_im),
            "C_re": _w(C_re),
            "C_im": _w(C_im),
            "log_dt": _w(log_dt),
            "Dskip": _w(Dskip),
        }
    _A_in = _A_setup()

    def _B_setup():
        B, S, D = 1, 2048, 576
        Df = D // 2 + 1
        return {
            "x": _w((lambda *s: torch.randn(*s, dtype=torch.bfloat16) * 0.05)(B, S, D)),
            "w_r": _w((lambda *s: torch.randn(*s, dtype=torch.bfloat16) * 1.6)(S, Df) * 0.02),
            "w_i": _w((lambda *s: torch.randn(*s, dtype=torch.bfloat16) * 1.6)(S, Df) * 0.02),
        }
    _B_in = _B_setup()

    return {
        "A_u": _A_in["u"],
        "A_A_re": _A_in["A_re"],
        "A_A_im": _A_in["A_im"],
        "A_B_re": _A_in["B_re"],
        "A_B_im": _A_in["B_im"],
        "A_C_re": _A_in["C_re"],
        "A_C_im": _A_in["C_im"],
        "A_log_dt": _A_in["log_dt"],
        "A_Dskip": _A_in["Dskip"],
        "B_x": _B_in["x"],
        "B_w_r": _B_in["w_r"],
        "B_w_i": _B_in["w_i"],
    }

def reference(A_u, A_A_re, A_A_im, A_B_re, A_B_im, A_C_re, A_C_im, A_log_dt, A_Dskip, B_x, B_w_r, B_w_i):
    def _A_ref(u, A_re, A_im, B_re, B_im, C_re, C_im, log_dt, Dskip):
        Bsz, L, D = u.shape
        dt = torch.exp(log_dt)[:, None]
        A = A_re + 1j * A_im
        Bc = B_re + 1j * B_im
        Cc = C_re + 1j * C_im
        logdA = dt * A
        dB = dt * Bc
        l = (lambda x, dt: x.to(dt))(torch.arange(L), A_re.dtype)
        P = torch.exp(logdA[:, :, None] * l[None, None, :])
        K = torch.einsum("dn,dnl,dn->dl", Cc, P, dB).real
        L2 = 2 * L
        Kp = _pad(K, ((0, 0), (0, L)))
        up = (lambda x, *d: x.permute(*d))(u, 0, 2, 1)
        up = _pad(up, ((0, 0), (0, 0), (0, L)))
        Kf = (lambda x, axes=(-2,-1): torch.fft.rfft2(x.float(), dim=axes))(Kp[:, None, :], axes=(-2, -1))
        Uf = (lambda x, axes=(-2,-1): torch.fft.rfft2(x.float(), dim=axes))(up[:, :, None, :], axes=(-2, -1))
        Yf = Uf * Kf[None, :, :, :]
        y = (lambda x, s, axes=(-2,-1): torch.fft.irfft2(x, s=s, dim=axes).to(torch.bfloat16))(Yf, (1, L2), axes=(-2, -1))[:, :, 0, :L]
        y = (lambda x, *d: x.permute(*d))(y, 0, 2, 1) + u * Dskip[None, None, :]
        return y

    def _B_ref(x, w_r, w_i):
        B, S, D = x.shape
        X = (lambda x, axes=(-2,-1): torch.fft.rfft2(x.float(), dim=axes))(x, axes=(-2, -1))
        W = w_r + 1j * w_i
        X = X * W
        y = (lambda x, s, axes=(-2,-1): torch.fft.irfft2(x, s=s, dim=axes).to(torch.bfloat16))(X, (S, D), axes=(-2, -1))
        return y

    a = _A_ref(A_u, A_A_re, A_A_im, A_B_re, A_B_im, A_C_re, A_C_im, A_log_dt, A_Dskip)
    b = _B_ref(B_x, B_w_r, B_w_i)
    return torch.concatenate([a, b], axis=-1)
```

```cpp
#include <hip/hip_runtime.h>
#include <stdint.h>
#define ROUND_L 1
#define ROUND_DT 1
typedef unsigned short u16;
__device__ inline float b2f(u16 x){ return __uint_as_float(((unsigned)x)<<16); }
__device__ inline u16 f2b(float f){ unsigned u=__float_as_uint(f); return (u16)((u+0x7FFF+((u>>16)&1))>>16); }
#define L 2048
#define D 576
#define N 128
#define DF 289
__global__ void kgen(const u16* Are,const u16* Aim,const u16* Bre,const u16* Bim,const u16* Cre,const u16* Cim,const u16* ldt,float* K){
  int d=blockIdx.y; int l=blockIdx.x*blockDim.x+threadIdx.x;
  float dt=expf(b2f(ldt[d]));
  if(ROUND_DT) dt=b2f(f2b(dt));
  float lf=(float)l; if(ROUND_L) lf=b2f(f2b(lf));
  float s=0;
  for(int n=0;n<N;n++){
    float ar=dt*b2f(Are[d*N+n]), ai=dt*b2f(Aim[d*N+n]);
    float br=dt*b2f(Bre[d*N+n]), bi=dt*b2f(Bim[d*N+n]);
    float cr=b2f(Cre[d*N+n]), ci=b2f(Cim[d*N+n]);
    float xr=cr*br-ci*bi, xi=cr*bi+ci*br;
    float e=expf(ar*lf); float sn,cs; sincosf(ai*lf,&sn,&cs);
    s+=e*(xr*cs-xi*sn);
  }
  K[d*L+l]=s;
}
__global__ void conv(const u16* u,const float* K,const u16* Ds,u16* out){
  int d=blockIdx.y; int t=blockIdx.x*blockDim.x+threadIdx.x;
  float s=0;
  for(int j=0;j<=t;j++) s+=K[d*L+t-j]*b2f(u[j*D+d]);
  float y=b2f(f2b(s));
  float sk=b2f(f2b(b2f(u[t*D+d])*b2f(Ds[d])));
  out[t*2*D+d]=f2b(y+sk);
}
__global__ void b1(const u16* x,float2* X1){
  int s=blockIdx.x; __shared__ float2 tw[D]; __shared__ float xr[D];
  for(int i=threadIdx.x;i<D;i+=blockDim.x){ float a,b; sincospif(-2.0f*i/D,&a,&b); tw[i]=make_float2(b,a); xr[i]=b2f(x[s*D+i]); }
  __syncthreads();
  for(int f=threadIdx.x;f<DF;f+=blockDim.x){ float re=0,im=0; int idx=0;
    for(int d=0;d<D;d++){ re+=xr[d]*tw[idx].x; im+=xr[d]*tw[idx].y; idx+=f; if(idx>=D) idx-=D; }
    X1[f*L+s]=make_float2(re,im);
  }
}
__global__ void b2(const float2* in,float2* out,const u16* wr,const u16* wi,int inv){
  int f=blockIdx.y; __shared__ float2 tw[L]; __shared__ float2 xs[L];
  for(int i=threadIdx.x;i<L;i+=blockDim.x){ float a,b; sincospif((inv?2.0f:-2.0f)*i/L,&a,&b); tw[i]=make_float2(b,a); xs[i]=in[f*L+i]; }
  __syncthreads();
  int k=blockIdx.x*blockDim.x+threadIdx.x;
  float re=0,im=0; int idx=0;
  for(int s=0;s<L;s++){ float2 w=tw[idx]; float2 v=xs[s]; re+=v.x*w.x-v.y*w.y; im+=v.x*w.y+v.y*w.x; idx=(idx+k)&(L-1);}
  if(!inv){ float a=b2f(wr[k*DF+f]), b=b2f(wi[k*DF+f]); float r2=re*a-im*b, i2=re*b+im*a; re=r2; im=i2; }
  out[f*L+k]=make_float2(re,im);
}
__global__ void b3(const float2* X,u16* out){
  int s=blockIdx.x; __shared__ float2 tw[D]; __shared__ float2 xs[DF];
  for(int i=threadIdx.x;i<D;i+=blockDim.x){ float a,b; sincospif(2.0f*i/D,&a,&b); tw[i]=make_float2(b,a); if(i<DF) xs[i]=X[i*L+s]; }
  __syncthreads();
  for(int d=threadIdx.x;d<D;d+=blockDim.x){ float acc=xs[0].x+((d&1)?-xs[288].x:xs[288].x); int idx=d;
    for(int f=1;f<288;f++){ acc+=2.f*(xs[f].x*tw[idx].x-xs[f].y*tw[idx].y); idx+=d; if(idx>=D) idx-=D; }
    out[s*2*D+D+d]=f2b(acc*(1.0f/(2048.f*576.f)));
  }
}
extern "C" void kernel_launch(void* const* d_in, const int* in_sizes, int n_in,
                              void* d_out, int out_size, void* d_ws, size_t ws_size,
                              hipStream_t stream) {
  const u16* const* in=(const u16* const*)d_in;
  float* K=(float*)d_ws; float2* X1=(float2*)((char*)d_ws+(8<<20)); float2* X2=(float2*)((char*)d_ws+(16<<20));
  u16* out=(u16*)d_out;
  kgen<<<dim3(L/256,D),256,0,stream>>>(in[1],in[2],in[3],in[4],in[5],in[6],in[7],K);
  conv<<<dim3(L/256,D),256,0,stream>>>(in[0],K,in[8],out);
  b1<<<L,256,0,stream>>>(in[9],X1);
  b2<<<dim3(L/256,DF),256,0,stream>>>(X1,X2,in[10],in[11],0);
  b2<<<dim3(L/256,DF),256,0,stream>>>(X2,X1,in[10],in[11],1);
  b3<<<L,256,0,stream>>>(X1,out);
}
```

```cpp
#include <hip/hip_runtime.h>

typedef unsigned u32;
typedef unsigned short u16;
typedef unsigned long long u64;
typedef __attribute__((ext_vector_type(2))) float f2;
typedef __attribute__((ext_vector_type(4))) float f4;
typedef __attribute__((ext_vector_type(16))) float f16v;
typedef __attribute__((ext_vector_type(2))) u32 u2;
typedef __attribute__((ext_vector_type(4))) u32 u4;
typedef __attribute__((ext_vector_type(2))) __bf16 h2;
typedef __attribute__((ext_vector_type(8))) __bf16 h8;
typedef __amdgpu_buffer_rsrc_t rsrc_t;
#define DI __device__ __forceinline__
#define LDSP(T, off) ((__attribute__((address_space(3))) T*)(lds + (off)))
typedef __attribute__((address_space(3))) char* ldsp_t;

#define STAMP(i)

DI u32 pk(float lo, float hi) { h2 v = {(__bf16)lo, (__bf16)hi}; return __builtin_bit_cast(u32, v); }
DI float asf(u32 x) { return __builtin_bit_cast(float, x); }
DI bool wall(bool p) { return __builtin_amdgcn_ballot_w64(!p) == 0; }
DI int imin(int a,int b){return a<b?a:b;}
DI int imax(int a,int b){return a>b?a:b;}
DI float blo(u32 x) { return asf(x << 16); }
DI float bhi(u32 x) { return asf(x & 0xffff0000u); }
DI float vsin(float r) { return __builtin_amdgcn_sinf(r); }
DI float vcos(float r) { return __builtin_amdgcn_cosf(r); }
DI u32 cs(float r) { return pk(vcos(r), vsin(r)); }
DI f16v mm32(u4 a, u4 b, f16v c) { return __builtin_amdgcn_mfma_f32_32x32x16_bf16(__builtin_bit_cast(h8, a), __builtin_bit_cast(h8, b), c, 0, 0, 0); }
DI f4 mm16(u4 a, u4 b, f4 c) { return __builtin_amdgcn_mfma_f32_16x16x32_bf16(__builtin_bit_cast(h8, a), __builtin_bit_cast(h8, b), c, 0, 0, 0); }
DI void bar() { asm volatile("s_waitcnt lgkmcnt(0)\n\ts_barrier" ::: "memory"); }
DI void drain() { asm volatile("s_waitcnt vmcnt(0)" ::: "memory"); }
DI rsrc_t mkr(const void* p, u32 n) { return __builtin_amdgcn_make_buffer_rsrc((void*)p, 0, n, 0x00020000); }
DI float swp(float v) { return __builtin_bit_cast(float, __builtin_amdgcn_mov_dpp(__builtin_bit_cast(int, v), 0xB1, 0xf, 0xf, true)); }
#define PIN(x) asm volatile("" : "+v"(x))

constexpr u32 SPINCAP = 1u << 16;
constexpr u32 T_OFF = 1u << 16;
constexpr u32 T_P = 129 * 128;
constexpr u32 FLAG_OFF = 4096;

struct Job { u4 c, cp; float ar[4], ai[4], br[4], bi[4]; };

DI void job_mats(Job& J, int g1, int n4, int g) {
  const float s1 = (float)(g1 + 18 * n4) * (1.f / 288.f);
#pragma unroll
  for (int i = 0; i < 4; i++) {
    J.c[i] = cs((float)(4 * g + i) * s1);
    J.cp[i] = cs((float)(n4 * (g1 + 18 * (4 * g + i))) * (1.f / 288.f));
  }
}
DI void job_coef(Job& J, ldsp_t lds, int g1, int g, int sel, int wo, int wp) {
  const float nrm = 0.5f / (2048.f * 288.f);
  const u32 sg = sel ? 0x80000000u : 0u;
#pragma unroll
  for (int r = 0; r < 4; r++) {
    const int gg = g1 + 18 * (4 * g + r);
    const int col = sel ? 576 - 2 * gg : 2 * gg;
    float o_r = blo(*LDSP(u16, wo + col));
    float o_i = asf(((u32)*LDSP(u16, wo + col + 16 * 608) << 16) ^ sg);
    if (r == 0) {
      const float a_r = blo(*LDSP(u16, wp + col));
      const float a_i = asf(((u32)*LDSP(u16, wp + col + 16 * 608) << 16) ^ sg ^ 0x80000000u);
      if (gg == 0) { o_r = 0.5f * (o_r + a_r); o_i = 0.5f * (o_i + a_i); }
    }
    const float p_r = swp(o_r), p_i = swp(o_i);
    const float th = (float)gg * (1.f / 576.f);
    const float s = vsin(th) * nrm, c = vcos(th) * nrm;
    const float d_r = o_r - p_r, d_i = o_i - p_i;
    J.ar[r] = (o_r + p_r) * nrm - s * d_r;
    J.ai[r] = (o_i + p_i) * nrm - s * d_i;
    J.br[r] = -c * d_i;
    J.bi[r] = c * d_r;
  }
}
DI u4 job_run(const Job& J, u4 a, u4 bre, u4 bim, u4 bp) {
  const f4 z = {0.f, 0.f, 0.f, 0.f};
  f4 re = mm16(a, bre, z), im = mm16(a, bim, z);
  u4 d0, d1;
#pragma unroll
  for (int r = 0; r < 4; r++) { d0[r] = pk(re[r], im[r]); d1[r] = pk(im[r], -re[r]); }
  re = mm16(J.c, d0, z); im = mm16(J.c, d1, z);
#pragma unroll
  for (int r = 0; r < 4; r++) {
    const float pr = swp(re[r]), pi = swp(im[r]);
    const float yr = J.ar[r] * re[r] - J.ai[r] * im[r] + J.br[r] * pr - J.bi[r] * pi;
    const float yi = J.ar[r] * im[r] + J.ai[r] * re[r] + J.br[r] * pi + J.bi[r] * pr;
    d0[r] = pk(yi, yr); d1[r] = pk(yr, -yi);
  }
  re = mm16(d0, J.cp, z); im = mm16(d1, J.cp, z);
#pragma unroll
  for (int r = 0; r < 4; r++) { d0[r] = pk(re[r], im[r]); d1[r] = pk(im[r], -re[r]); }
  re = mm16(bp, d0, z); im = mm16(bp, d1, z);
#pragma unroll
  for (int r = 0; r < 4; r++) d0[r] = pk(re[r], im[r]);
  return d0;
}

constexpr int B_XB = 0, B_MA = 23040, B_MB = B_MA + 6144, B_WB = B_MB + 4096, B_J3 = B_WB + 19456, B_P3 = B_WB;

DI void role_b(ldsp_t lds, int b, const u16* x, const u16* wr, const u16* wi, u16* out, char* ws, u64* st) {
  const int tid = __builtin_amdgcn_workitem_id_x(), l = tid & 63, w = __builtin_amdgcn_readfirstlane(tid >> 6);
  const int n5 = l & 31, h = l >> 5, n4 = l & 15, g = l >> 4, jj = g & 1;
  const int s2 = b & 7, c = b >> 3, p = s2 * 18 + c;
  const f16v z16 = {0.f, 0.f, 0.f, 0.f, 0.f, 0.f, 0.f, 0.f, 0.f, 0.f, 0.f, 0.f, 0.f, 0.f, 0.f, 0.f};
  u4 xv[2];
  {
    const rsrc_t rx = mkr(x, 2048 * 1152);
    const u32 vo = h * 4 * 147456 + jj * 9216 + n4 * 4, so = s2 * 1152 + c * 64 + w * 18432;
#pragma unroll
    for (int i = 0; i < 8; i++) xv[i >> 2][i & 3] = __builtin_amdgcn_raw_buffer_load_b32(rx, vo, so + ((i >> 2) * 8 + (i & 3)) * 147456, 0);
  }
  const rsrc_t rw = mkr(ws, 4u << 20);
  u32 tag = __builtin_amdgcn_raw_buffer_load_b32(rw, 0, 0, 16);
  const int kap = b - 15;
  const int k1 = kap < 112 ? (kap >> 4) + 1 + 16 * (kap & 15) : (kap < 120 ? 8 + 16 * (kap - 112) : 16 * (kap - 120));
  u4 wv[4];
  if (kap >= 0) {
    const rsrc_t r0 = mkr(wr, 2048 * 578), r1 = mkr(wi, 2048 * 578);
    const int ka_ = k1 + 256 * w, kb_ = (2048 - ka_) & 2047;
    const u32 vo = l < 37 ? l * 16 : 0x7ffffff0u;
    wv[0] = __builtin_amdgcn_raw_buffer_load_b128(r0, vo, (ka_ * 578) & ~15, 0);
    wv[1] = __builtin_amdgcn_raw_buffer_load_b128(r0, vo, (kb_ * 578) & ~15, 0);
    wv[2] = __builtin_amdgcn_raw_buffer_load_b128(r1, vo, (ka_ * 578) & ~15, 0);
    wv[3] = __builtin_amdgcn_raw_buffer_load_b128(r1, vo, (kb_ * 578) & ~15, 0);
  }
  __builtin_amdgcn_sched_barrier(0);
  const int comp = n5 & 1, idx = 2 * (((n5 >> 1) & 1) + 2 * (n5 >> 3)) + ((n5 >> 2) & 1);
  const float q4 = comp ? 0.25f : 0.f;
  u4 m1[2], m2[2];
  const int kaw = w ? w : 8;
  {
    const float a1 = (float)idx * (1.f / 16.f), o1 = idx >= 9 ? -q4 : q4, a2 = (float)(kaw + 16 * idx) * (1.f / 256.f);
#pragma unroll
    for (int i = 0; i < 8; i++) {
      const float j = (float)((i >> 2) * 8 + (i & 3) + 4 * h);
      m1[i >> 2][i & 3] = cs(j * a1 + o1);
      m2[i >> 2][i & 3] = cs(j * a2 + q4);
    }
  }
  Job J0, J1;
  if (kap >= 0) {
    u4 f;
    if (w < 6) {
      const int T = w >= 3, t = w - 3 * T;
      const int g1 = idx + 16 * T;
#pragma unroll
      for (int i = 0; i < 4; i++) { const int ii = 4 * t + i; f[i] = ii <= 8 ? cs((float)((2 * ii + h) * g1) * (1.f / 18.f) + q4) : 0u; }
      *LDSP(u4, B_MA + w * 1024 + l * 16) = f;
    } else {
      u4 f2_;
#pragma unroll
      for (int i = 0; i < 2; i++) {
        const int u = 2 * g + i, v = n4 >> 1;
        const int e = w == 6 ? u * (k1 + 256 * v) : v * (k1 + 256 * u);
        const float a = (float)(e & 2047) * (1.f / 2048.f), cc = vcos(a), ss = vsin(a);
        const u32 re = pk(cc, ss), im = pk(-ss, cc);
        f[2 * i] = (n4 & 1) ? 0u : re; f[2 * i + 1] = (n4 & 1) ? re : 0u;
        f2_[2 * i] = (n4 & 1) ? 0u : im; f2_[2 * i + 1] = (n4 & 1) ? im : 0u;
      }
      *LDSP(u4, B_MB + (w - 6) * 2048 + l * 16) = f;
      *LDSP(u4, B_MB + (w - 6) * 2048 + 1024 + l * 16) = f2_;
    }
  }
  PIN(m1[0]); PIN(m1[1]); PIN(m2[0]); PIN(m2[1]);
  STAMP(1);
  const int wadr = B_XB + (h * 16 + n4) * 80 + (2 * w + jj) * 4;
  {
    f16v a = mm32(m1[0], xv[0], z16);
    a = mm32(m1[1], xv[1], a);
    STAMP(2);
#pragma unroll
    for (int q = 0; q < 8; q++) *LDSP(u32, wadr + q * 2560) = pk(a[2 * q], a[2 * q + 1]);
  }
  bar();
  {
    const int kr = jj ? 16 - kaw : kaw;
    const u32 sg = (w == 0 && jj) ? 0x80000000u : 0u;
    u4 d0 = *LDSP(u4, B_XB + (kr * 16 + n4) * 80 + h * 16), d1 = *LDSP(u4, B_XB + (kr * 16 + n4) * 80 + h * 16 + 32);
    const u32 so = T_OFF + p * T_P + (kaw - 1) * 2048;
    if (w == 0) {
      d0 ^= sg; d1 ^= sg;
      u4 e0 = *LDSP(u4, B_XB + n4 * 80 + h * 16), e1 = *LDSP(u4, B_XB + n4 * 80 + h * 16 + 32);
      e0 ^= sg; e1 ^= sg;
      f16v a = mm32(m2[0], d0, z16), a0 = mm32(m1[0], e0, z16);
      a = mm32(m2[1], d1, a); a0 = mm32(m1[1], e1, a0);
#pragma unroll
      for (int q = 0; q < 4; q++) __builtin_amdgcn_raw_buffer_store_b32(pk(a[2 * q], a[2 * q + 1]) & ~1u, rw, l * 4 + q * 256, so, 16);
#pragma unroll
      for (int q = 0; q < 4; q++) __builtin_amdgcn_raw_buffer_store_b32(pk(a0[2 * q], a0[2 * q + 1]) & ~1u, rw, l * 4 + q * 256 + 1024, so, 16);
      __builtin_amdgcn_raw_buffer_store_b32(pk(a0[8], a0[9]) & ~1u, rw, h ? 0x7ffffff0u : l * 4 + 2048, so, 16);
    } else {
      f16v a = mm32(m2[0], d0, z16);
      a = mm32(m2[1], d1, a);
#pragma unroll
      for (int q = 0; q < 8; q++) __builtin_amdgcn_raw_buffer_store_b32(pk(a[2 * q], a[2 * q + 1]) & ~1u, rw, l * 4 + q * 256, so, 16);
    }
  }
  STAMP(3);
  __builtin_amdgcn_sched_barrier(0);
  tag = tag * 1664525u + 1013904223u;
  if (kap >= 0) {
    if (l < 37) {
#pragma unroll
      for (int i = 0; i < 4; i++) *LDSP(u4, B_WB + ((i >> 1) * 16 + 2 * w + (i & 1)) * 608 + l * 16) = wv[i];
    }
    job_mats(J0, w, n4, g);
    job_mats(J1, w + 8, n4, g);
    PIN(J0.c); PIN(J0.cp); PIN(J1.c); PIN(J1.cp);
  }
  drain();
  bar();
  if (tid == 0) __builtin_amdgcn_raw_buffer_store_b32(tag, rw, 0, FLAG_OFF + p * 128, 16);
  STAMP(4);
  if (kap >= 0) {
    {
      const int sel = n4 & 1, kk = k1 + 256 * (n4 >> 1), kn = (2048 - kk) & 2047;
      const int wo = B_WB + n4 * 608 + (((sel ? kn : kk) * 578) & 15), wp = B_WB + (n4 ^ 1) * 608 + (((sel ? kk : kn) * 578) & 15);
      job_coef(J0, lds, w, g, sel, wo, wp);
      job_coef(J1, lds, w + 8, g, sel, wo, wp);
      if (w >= 6) {
        Job J2;
        job_mats(J2, w + 10, n4, g);
        job_coef(J2, lds, w + 10, g, sel, wo, wp);
        const int o = B_J3 + (w - 6) * 6144 + l * 16;
        *LDSP(u4, o) = J2.c; *LDSP(u4, o + 1024) = J2.cp;
        *LDSP(f4, o + 2048) = (f4){J2.ar[0], J2.ar[1], J2.ar[2], J2.ar[3]};
        *LDSP(f4, o + 3072) = (f4){J2.ai[0], J2.ai[1], J2.ai[2], J2.ai[3]};
        *LDSP(f4, o + 4096) = (f4){J2.br[0], J2.br[1], J2.br[2], J2.br[3]};
        *LDSP(f4, o + 5120) = (f4){J2.bi[0], J2.bi[1], J2.bi[2], J2.bi[3]};
      }
#pragma unroll
      for (int r = 0; r < 4; r++) { PIN(J0.ar[r]); PIN(J0.ai[r]); PIN(J0.br[r]); PIN(J0.bi[r]); PIN(J1.ar[r]); PIN(J1.ai[r]); PIN(J1.br[r]); PIN(J1.bi[r]); }
    }
    STAMP(5);
    _Pragma("nounroll") for (u32 sp = 0; sp < SPINCAP; sp++) {
      const u32 f = __builtin_amdgcn_raw_buffer_load_b32(rw, (l < 18 ? l : 0) * 128, FLAG_OFF + w * 18 * 128, 16);
      if (wall(f == tag)) break;
      asm volatile("" ::: "memory");
    }
    asm volatile("" ::: "memory");
    STAMP(6);
    const u32 vo = h * T_P + n5 * 4, so = T_OFF + w * 18 * T_P + kap * 128;
    {
      u4 v0, v1, v2 = {0u, 0u, 0u, 0u};
#pragma unroll
      for (int i = 0; i < 4; i++) v0[i] = __builtin_amdgcn_raw_buffer_load_b32(rw, vo, so + 2 * i * T_P, 16);
#pragma unroll
      for (int i = 0; i < 4; i++) v1[i] = __builtin_amdgcn_raw_buffer_load_b32(rw, vo, so + 2 * (i + 4) * T_P, 16);
      v2[0] = __builtin_amdgcn_raw_buffer_load_b32(rw, vo, so + 16 * T_P, 16);
      f16v a = mm32(*LDSP(u4, B_MA + l * 16), v0, z16), y = mm32(*LDSP(u4, B_MA + 3072 + l * 16), v0, z16);
      a = mm32(*LDSP(u4, B_MA + 1024 + l * 16), v1, a); y = mm32(*LDSP(u4, B_MA + 4096 + l * 16), v1, y);
      a = mm32(*LDSP(u4, B_MA + 2048 + l * 16), v2, a); y = mm32(*LDSP(u4, B_MA + 5120 + l * 16), v2, y);
      STAMP(7);
#pragma unroll
      for (int q = 0; q < 8; q++) *LDSP(u32, wadr + q * 2560) = pk(a[2 * q], a[2 * q + 1]);
      *LDSP(u32, wadr + 8 * 2560) = pk(y[0], y[1]);
    }
    const u4 bre = *LDSP(u4, B_MB + l * 16), bim = *LDSP(u4, B_MB + 1024 + l * 16), bp = *LDSP(u4, B_MB + 2048 + l * 16);
    bar();
    STAMP(8);
    if (kap == 0 && tid == 0) __builtin_amdgcn_raw_buffer_store_b32(tag, rw, 0, 0, 16);
    {
      const int ja = B_XB + (w * 16 + n4) * 80 + g * 16;
      const u4 a0 = *LDSP(u4, ja), a1 = *LDSP(u4, ja + 8 * 1280);
      const u4 r0 = job_run(J0, a0, bre, bim, bp), r1 = job_run(J1, a1, bre, bim, bp);
      *LDSP(u4, ja) = r0; *LDSP(u4, ja + 8 * 1280) = r1;
      if (w >= 6) {
        Job J2;
        const int o = B_J3 + (w - 6) * 6144 + l * 16;
        J2.c = *LDSP(u4, o); J2.cp = *LDSP(u4, o + 1024);
        const f4 t0 = *LDSP(f4, o + 2048), t1 = *LDSP(f4, o + 3072), t2 = *LDSP(f4, o + 4096), t3 = *LDSP(f4, o + 5120);
#pragma unroll
        for (int r = 0; r < 4; r++) { J2.ar[r] = t0[r]; J2.ai[r] = t1[r]; J2.br[r] = t2[r]; J2.bi[r] = t3[r]; }
        *LDSP(u4, ja + 10 * 1280) = job_run(J2, *LDSP(u4, ja + 10 * 1280), bre, bim, bp);
      }
    }
    bar();
    STAMP(9);
    {
      u4 v0, v1, v2 = {0u, 0u, 0u, 0u};
#pragma unroll
      for (int i = 0; i < 4; i++) { v0[i] = *LDSP(u32, wadr + i * 2560); v1[i] = *LDSP(u32, wadr + (i + 4) * 2560); }
      v2[0] = *LDSP(u32, wadr + 8 * 2560);
      f16v a = mm32(*LDSP(u4, B_MA + l * 16), v0, z16), y = mm32(*LDSP(u4, B_MA + 3072 + l * 16), v0, z16);
      a = mm32(*LDSP(u4, B_MA + 1024 + l * 16), v1, a); y = mm32(*LDSP(u4, B_MA + 4096 + l * 16), v1, y);
      a = mm32(*LDSP(u4, B_MA + 2048 + l * 16), v2, a); y = mm32(*LDSP(u4, B_MA + 5120 + l * 16), v2, y);
#pragma unroll
      for (int q = 0; q < 8; q++) __builtin_amdgcn_raw_buffer_store_b32(pk(a[2 * q], a[2 * q + 1]) | 1u, rw, vo, so + 2 * q * T_P, 16);
      __builtin_amdgcn_raw_buffer_store_b32(pk(y[0], y[1]) | 1u, rw, vo, so + 16 * T_P, 16);
    }
    STAMP(10);
  }
  __builtin_amdgcn_sched_barrier(0);
  u4 m3[2], m30[2], m4[2];
  u32 la[8];
#pragma unroll
  for (int i = 0; i < 8; i++) {
    const int kb = 2 * i + h;
    float a = (float)(idx * (kaw + 16 * kb)) * (1.f / 256.f) + q4;
    if (w == 0 && i >= 4) a = 0.5f - a;
    m3[i >> 2][i & 3] = cs(a);
    const int ka = (i >> 2) * 8 + (i & 3) + 4 * h;
    float a4 = (float)(idx * ka) * (1.f / 16.f) + q4;
    if (ka >= 9) a4 = 0.5f - a4;
    m4[i >> 2][i & 3] = cs(a4);
    la[i] = l * 4 + i * 256;
  }
  if (w == 0) {
#pragma unroll
    for (int i = 0; i < 8; i++) {
      const int kb = 2 * i + h;
      float a = (float)(idx * kb) * (1.f / 16.f) + q4;
      if (kb >= 9) a = 0.5f - a;
      m30[i >> 2][i & 3] = cs(a);
      const int ln = jj ? (kb <= 8 ? 8 + kb : 24 - kb) : (kb <= 7 ? kb : 15 - kb);
      la[i] = ln * 128 + (kb > 7 + jj ? 64 : 0) + n4 * 4;
    }
  }
  PIN(m3[0]); PIN(m3[1]); PIN(m4[0]); PIN(m4[1]);
  drain();
  STAMP(11);
  {
    const u32 so = T_OFF + p * T_P + (kaw - 1) * 2048;
    u4 v0, v1;
    _Pragma("nounroll") for (u32 sp = 0; sp < SPINCAP; sp++) {
#pragma unroll
      for (int i = 0; i < 4; i++) v0[i] = __builtin_amdgcn_raw_buffer_load_b32(rw, la[i], so, 16);
#pragma unroll
      for (int i = 0; i < 4; i++) v1[i] = __builtin_amdgcn_raw_buffer_load_b32(rw, la[i + 4], so, 16);
      const u4 m = v0 & v1;
      if (wall(m[0] & m[1] & m[2] & m[3] & 1u)) break;
      if (kap < 0) __builtin_amdgcn_s_sleep(4);
      asm volatile("" ::: "memory");
    }
    STAMP(12);
    f16v a;
    int kr = jj ? 16 - kaw : kaw;
    if (w == 0) {
      const u32 mk = jj ? 0u : ~0u;
      a = mm32(m3[0], v0 & mk, z16);
      a = mm32(m3[1], v1 & mk, a);
      a = mm32(m30[0], v0 & ~mk, a);
      a = mm32(m30[1], v1 & ~mk, a);
      kr = jj ? 0 : 8;
    } else {
      a = mm32(m3[0], v0, z16);
      a = mm32(m3[1], v1, a);
    }
    const int wa = B_P3 + (h * 16 + n4) * 80 + kr * 4;
#pragma unroll
    for (int q = 0; q < 8; q++) *LDSP(u32, wa + q * 2560) = pk(a[2 * q], a[2 * q + 1]);
  }
  bar();
  STAMP(13);
  {
    const int ra = B_P3 + ((2 * w + jj) * 16 + n4) * 80 + h * 16;
    f16v a = mm32(m4[0], *LDSP(u4, ra), z16);
    a = mm32(m4[1], *LDSP(u4, ra + 32), a);
    const rsrc_t ro = mkr(out, 2048 * 2304);
    const u32 vo = h * 294912 + jj * 18432 + n4 * 4, so = s2 * 2304 + 1152 + c * 64 + w * 36864;
#pragma unroll
    for (int q = 0; q < 8; q++) __builtin_amdgcn_raw_buffer_store_b32(pk(a[2 * q + 1], a[2 * q]), ro, vo, so + q * 589824, 0);
  }
  STAMP(14);
}

constexpr int A_PB = 8704, A_TAB = 8 * A_PB, A_TS = 3072;
constexpr int A_U = 3072 + 512;
constexpr int TE = 8, TO = TE + 303, T2A = 608, T2B = 656, T3 = 704;

struct cf { float r, i; };
DI cf cmul(cf a, cf b) { return {a.r * b.r - a.i * b.i, a.r * b.i + a.i * b.r}; }
DI cf cexpo(float lr, float rev) { const float m = __builtin_amdgcn_exp2f(lr), f = __builtin_amdgcn_fractf(rev); return {m * vcos(f), m * vsin(f)}; }
DI u4 boxf(int n4, int g, int off, int wd) {
  const u32 m = (((1u << (2 * wd + 1)) - 1u) << 20) >> imin(imax(20 - (16 + n4 - 8 * g - off - wd), 0), 31);
  u4 r;
#pragma unroll
  for (int i = 0; i < 4; i++) r[i] = ((((m >> (2 * i)) & 3u) * 0x8001u) & 0x10001u) * 0x3f80u;
  return r;
}
DI u4 rd4(ldsp_t lds, int a) {
  return (u4){*LDSP(u32, a), *LDSP(u32, a - 4), *LDSP(u32, a - 8), *LDSP(u32, a - 12)};
}
DI u4 pk4(f4 a, f4 b) { return (u4){pk(a[0], b[0]), pk(a[1], b[1]), pk(a[2], b[2]), pk(a[3], b[3])}; }

template <int I0, int I1>
DI void conv(ldsp_t lds, int ua, int fa, int ta, const u4& b3, const u4& b5, const u4& b7, const u4& b9, int ya) {
  const f4 z = {0.f, 0.f, 0.f, 0.f};
  f4 acc[I1 - I0];
#pragma unroll
  for (int i = 0; i < I1 - I0; i++) acc[i] = z;
  constexpr int U0 = I0 > 0 ? I0 - 1 : 0;
#pragma unroll 2
  for (int s = 0; s < 8; s++) {
    const u4 f0 = rd4(lds, fa + s * 64), f1 = rd4(lds, fa + s * 64 + 512);
#pragma unroll
    for (int i = U0; i < I1; i++) {
      const u4 u = *LDSP(u4, ua + i * 512 - s * 64);
      if (i >= I0) acc[i - I0] = mm16(f0, u, acc[i - I0]);
      if (i + 1 < I1) acc[i + 1 - I0] = mm16(f1, u, acc[i + 1 - I0]);
    }
  }
  const u4 f16_ = rd4(lds, fa + 16 * 64), f17 = rd4(lds, fa + 17 * 64);
#pragma unroll
  for (int j = 0; j < 6; j++) {
    const bool s2 = (j + 2 < I1) && (j + 4 >= I0), s3 = (j + 4 < I1);
    if (!s2 && !s3) continue;
    const u4 x = *LDSP(u4, ua + j * 512), y = *LDSP(u4, ua + j * 512 - 16);
    if (j + 2 >= I0 && j + 2 < I1) acc[j + 2 - I0] = mm16(f16_, x, acc[j + 2 - I0]);
    if (j + 4 >= I0 && j + 4 < I1) acc[j + 4 - I0] = mm16(f17, x, acc[j + 4 - I0]);
    if (s2) {
      const u4 pa = pk4(mm16(x, b3, z), mm16(x, b5, z)), pb = pk4(mm16(y, b3, z), mm16(y, b5, z));
#pragma unroll
      for (int d = 0; d < 3; d++) {
        const int i = j + 2 + d;
        if (i >= I0 && i < I1) {
          acc[i - I0] = mm16(pa, rd4(lds, ta + T2A * 4 + d * 64), acc[i - I0]);
          acc[i - I0] = mm16(pb, rd4(lds, ta + T2B * 4 + d * 64), acc[i - I0]);
        }
      }
    }
    if (s3) {
      const u4 pc = pk4(mm16(x, b7, z), mm16(y, b9, z));
#pragma unroll
      for (int d = 0; d < 4; d++) {
        const int i = j + 4 + d;
        if (i >= I0 && i < I1) acc[i - I0] = mm16(pc, rd4(lds, ta + T3 * 4 + d * 64), acc[i - I0]);
      }
    }
  }
#pragma unroll
  for (int i = 0; i < I1 - I0; i++) *LDSP(u2, ya + i * 512) = (u2){pk(acc[i][0], acc[i][1]), pk(acc[i][2], acc[i][3])};
}

DI void role_a(ldsp_t lds, int a, const u16* u, const u16* Are, const u16* Aim, const u16* Bre, const u16* Bim, const u16* Cre,
               const u16* Cim, const u16* ldt, const u16* dsk, u16* out, u64* st) {
  const int tid = __builtin_amdgcn_workitem_id_x(), l = tid & 63, w = __builtin_amdgcn_readfirstlane(tid >> 6);
  const int n5 = l & 31, h = l >> 5, n4 = l & 15, g = l >> 4;
  const int xc = a & 7, i0 = a >> 3, grp = i0 < 16 ? xc * 16 + i0 : 128 + 2 * xc + (i0 - 16);
  const int ch = w & 3, hf = w >> 2, d = grp * 4 + ch;
  const int pi_ = d * 128 + hf * 64 + l;
  const u32 vare = Are[pi_], vaim = Aim[pi_], vbre = Bre[pi_], vbim = Bim[pi_], vcre = Cre[pi_], vcim = Cim[pi_], vldt = ldt[d];
  const int t4 = (tid + i0 * 28) & 511;
  u2 uv[4];
  {
    const rsrc_t ru = mkr(u, 2048 * 1152);
#pragma unroll
    for (int i = 0; i < 4; i++) uv[i] = __builtin_amdgcn_raw_buffer_load_b64(ru, t4 * 4608 + i * 1152, grp * 8, 0);
  }
  const u2 dv = *(const u2*)(dsk + grp * 4);
  __builtin_amdgcn_sched_barrier(0);
  if (tid < 384) { *LDSP(u4, A_TAB + tid * 32) = (u4){0u, 0u, 0u, 0u}; *LDSP(u4, A_TAB + tid * 32 + 16) = (u4){0u, 0u, 0u, 0u}; }
  u4 b3 = boxf(n4, g, 4, 1), b5 = boxf(n4, g, 8, 2), b7 = boxf(n4, g, 8, 3), b9 = boxf(n4, g, 8, 4);
  PIN(b3); PIN(b5); PIN(b7); PIN(b9);
  STAMP(1);
  const int pb = w * A_PB;
  f16v kacc;
  {
    const float dt = blo(pk(expf(blo(vldt)), 0.f));
    const float lr = dt * blo(vare) * 1.44269504088896341f, rv = dt * blo(vaim) * 0.15915494309189535f;
    const cf c0 = cmul((cf){blo(vcre), blo(vcim)}, (cf){dt * blo(vbre), dt * blo(vbim)});
    STAMP(2);
    const cf w1 = cexpo(lr, rv), w8 = cexpo(8.f * lr, 8.f * rv), V = cexpo(256.f * lr, 256.f * rv);
    const int wa = pb + l * 4;
#define PUTG(row, v) *LDSP(u32, wa + (row) * 272) = pk((v).r, -(v).i)
    cf gk = c0, t, t4_, t8;
    PUTG(0, gk); t = cmul(gk, V); PUTG(9, t);
    gk = cmul(gk, w1); PUTG(1, gk);
    gk = cmul(gk, w1); PUTG(2, gk); t = cmul(gk, V); PUTG(10, t);
    gk = cmul(gk, w1); PUTG(3, gk);
    gk = cmul(gk, w1); PUTG(4, gk); t4_ = cmul(gk, V); PUTG(11, t4_);
    gk = cmul(gk, w1); PUTG(5, gk); t4_ = cmul(t4_, V); PUTG(14, t4_);
    gk = cmul(gk, w1); PUTG(6, gk); t = cmul(gk, V); PUTG(12, t);
    gk = cmul(gk, w1); PUTG(7, gk); t4_ = cmul(t4_, V); PUTG(16, t4_);
    gk = cmul(gk, w1); PUTG(8, gk);
    t8 = cmul(gk, V); PUTG(13, t8);
    t8 = cmul(t8, V); PUTG(15, t8);
    t8 = cmul(t8, V); PUTG(17, t8);
    t8 = cmul(t8, V); PUTG(18, t8);
    t8 = cmul(t8, V); PUTG(19, t8);
    t8 = cmul(t8, V); PUTG(20, t8);
    t8 = cmul(t8, V); PUTG(21, t8);
    const int reg = (n5 & 3) + 4 * (n5 >> 3), hh = (n5 >> 2) & 1;
    const int grow = reg >= 12 ? 14 + (reg & 1) + 2 * hh + 2 * (reg & 2) : (reg > 8 ? 0 : (hh ? 9 + (int)((0x443222100ull >> (4 * reg)) & 15) : reg));
    u4 gf[8];
    asm volatile("" ::: "memory");
#pragma unroll
    for (int t_ = 0; t_ < 8; t_++) gf[t_] = *LDSP(u4, pb + grow * 272 + h * 16 + t_ * 32);
    asm volatile("" ::: "memory");
    const cf w16 = cmul(w8, w8);
    cf he = {1.f, 0.f}, ho = w8;
#pragma unroll
    for (int q = 0; q < 32; q += 2) {
      *LDSP(u32, wa + q * 272) = pk(he.r, he.i);
      *LDSP(u32, wa + (q + 1) * 272) = pk(ho.r, ho.i);
      if (q == 0) he = w16; else if (q < 30) he = cmul(he, w16);
      if (q < 30) ho = cmul(ho, w16);
    }
    asm volatile("" ::: "memory");
    STAMP(3);
    kacc = (f16v){0.f, 0.f, 0.f, 0.f, 0.f, 0.f, 0.f, 0.f, 0.f, 0.f, 0.f, 0.f, 0.f, 0.f, 0.f, 0.f};
#pragma unroll
    for (int t_ = 0; t_ < 8; t_++) kacc = mm32(gf[t_], *LDSP(u4, pb + n5 * 272 + h * 16 + t_ * 32), kacc);
    asm volatile("" ::: "memory");
  }
  if (hf) {
    *LDSP(f4, pb + l * 16) = (f4){kacc[0], kacc[1], kacc[2], kacc[3]};
    *LDSP(f4, pb + 1024 + l * 16) = (f4){kacc[4], kacc[5], kacc[6], kacc[7]};
    *LDSP(float, pb + 2048 + l * 4) = kacc[8];
  } else {
    *LDSP(f4, pb + l * 16) = (f4){kacc[12], kacc[13], kacc[14], kacc[15]};
  }
  STAMP(4);
  bar();
  STAMP(5);
  const int tb = A_TAB + ch * A_TS;
  {
    const int po = (w ^ 4) * A_PB;
    if (hf) {
      const f4 v = *LDSP(f4, po + l * 16);
      const u32 p0 = pk(kacc[12] + v[0], kacc[13] + v[1]), p1 = pk(kacc[14] + v[2], kacc[15] + v[3]);
      *LDSP(u32, tb + (((n5 & 1) ? T2B : T2A) + 16 * h + (n5 >> 1)) * 4) = p0;
      *LDSP(u16, tb + T3 * 4 + (64 * h + n5) * 2) = (u16)p1;
      *LDSP(u16, tb + T3 * 4 + (64 * h + 32 + n5) * 2) = (u16)(p1 >> 16);
      if (l == 63) {
        *LDSP(u32, tb + (TE + 273) * 4) = p0 >> 16;
        *LDSP(u32, tb + (TE + 274) * 4) = p0 & 0xffff0000u;
        *LDSP(u32, tb + (TO + 274) * 4) = (p0 >> 16) | (p0 & 0xffff0000u);
      }
      if (l < 32) *LDSP(u4, ch * A_PB + A_U - 512 + l * 16) = (u4){0u, 0u, 0u, 0u};
    } else {
      const f4 v0 = *LDSP(f4, po + l * 16), v1 = *LDSP(f4, po + 1024 + l * 16);
      const float v2 = *LDSP(float, po + 2048 + l * 4);
      float k[9];
#pragma unroll
      for (int i = 0; i < 4; i++) { k[i] = kacc[i] + v0[i]; k[i + 4] = kacc[i + 4] + v1[i]; }
      k[8] = kacc[8] + v2;
      const u4 e = {pk(k[1], k[0]), pk(k[3], k[2]), pk(k[5], k[4]), pk(k[7], k[6])};
      const u4 o = {pk(k[2], k[1]), pk(k[4], k[3]), pk(k[6], k[5]), pk(k[8], k[7])};
      *LDSP(u4, tb + TE * 4 + l * 16) = e;
      *LDSP(u4, tb + (TO + 1) * 4 + l * 16) = o;
      if (l == 0) *LDSP(u32, tb + TO * 4) = e[0] >> 16;
      if (l == 63) {
        *LDSP(u32, tb + (TE + 256) * 4) = o[3];
        *LDSP(u32, tb + (TE + 257) * 4) = o[3] & 0xffff0000u;
        *LDSP(u32, tb + (TO + 257) * 4) = o[3];
      }
    }
#pragma unroll
    for (int cc = 0; cc < 4; cc++) {
      const u32 sel = (cc & 1) ? 0x07060302u : 0x05040100u;
      *LDSP(u2, cc * A_PB + A_U + t4 * 8) = (u2){__builtin_amdgcn_perm(uv[1][cc >> 1], uv[0][cc >> 1], sel), __builtin_amdgcn_perm(uv[3][cc >> 1], uv[2][cc >> 1], sel)};
    }
  }
  STAMP(6);
  bar();
  STAMP(7);
  {
    const int ua = ch * A_PB + A_U + n4 * 32 + g * 16 - 32;
    const int fa = tb + (((n4 & 1) ? TE : TO) + ((16 + n4 - 8 * g) >> 1)) * 4;
    const int ta = tb + (n4 - 4 * g) * 4;
    const int ya = pb + n4 * 32 + g * 8;
    if (hf) conv<5, 8>(lds, ua, fa, ta, b3, b5, b7, b9, ya);
    else conv<0, 5>(lds, ua, fa, ta, b3, b5, b7, b9, ya);
  }
  STAMP(8);
  bar();
  STAMP(9);
  {
    const int yo = t4 < 320 ? t4 * 8 : 4 * A_PB + (t4 - 320) * 8;
    u2 y[4];
#pragma unroll
    for (int cc = 0; cc < 4; cc++) y[cc] = *LDSP(u2, cc * A_PB + yo);
    const rsrc_t ro = mkr(out, 2048 * 2304);
    const float d0 = blo(dv[0]), d1 = bhi(dv[0]), d2 = blo(dv[1]), d3 = bhi(dv[1]);
#pragma unroll
    for (int i = 0; i < 4; i++) {
      const u32 s0 = pk(blo(uv[i][0]) * d0, bhi(uv[i][0]) * d1), s1 = pk(blo(uv[i][1]) * d2, bhi(uv[i][1]) * d3);
      float yy[4];
#pragma unroll
      for (int cc = 0; cc < 4; cc++) yy[cc] = (i & 1) ? bhi(y[cc][i >> 1]) : blo(y[cc][i >> 1]);
      const u2 o = {pk(yy[0] + blo(s0), yy[1] + bhi(s0)), pk(yy[2] + blo(s1), yy[3] + bhi(s1))};
      __builtin_amdgcn_raw_buffer_store_b64(o, ro, t4 * 9216 + i * 2304, grp * 8, 0);
    }
  }
  STAMP(10);
}

#if defined(__HIP_DEVICE_COMPILE__)
__attribute__((target("no-packed-fp32-ops")))
#endif
__global__ void __launch_bounds__(512, 4) fused_kernel(const u16* __restrict__ u, const u16* __restrict__ Are, const u16* __restrict__ Aim,
    const u16* __restrict__ Bre, const u16* __restrict__ Bim, const u16* __restrict__ Cre, const u16* __restrict__ Cim,
    const u16* __restrict__ ldt, const u16* __restrict__ dsk, const u16* __restrict__ x, const u16* __restrict__ wr,
    const u16* __restrict__ wi, u16* out, char* ws) {
  __shared__ __attribute__((aligned(16))) char smem[81920];
  ldsp_t lds = (ldsp_t)smem;
  const int b = __builtin_amdgcn_workgroup_id_x();
  u64* st = nullptr;
  if (b < 144) role_b(lds, b, x, wr, wi, out, ws, st);
  else role_a(lds, b - 144, u, Are, Aim, Bre, Bim, Cre, Cim, ldt, dsk, out, st);
}

extern "C" void kernel_launch(void* const* d_in, const int* in_sizes, int n_in, void* d_out, int out_size, void* d_ws, size_t ws_size,
                              hipStream_t stream) {
  const u16* const* in = (const u16* const*)d_in;
  fused_kernel<<<288, 512, 0, stream>>>(in[0], in[1], in[2], in[3], in[4], in[5], in[6], in[7], in[8], in[9], in[10], in[11],
                                        (u16*)d_out, (char*)d_ws);
}
```

```cpp
#define PROF
#include <hip/hip_runtime.h>

typedef unsigned u32;
typedef unsigned short u16;
typedef unsigned long long u64;
typedef __attribute__((ext_vector_type(2))) float f2;
typedef __attribute__((ext_vector_type(4))) float f4;
typedef __attribute__((ext_vector_type(16))) float f16v;
typedef __attribute__((ext_vector_type(2))) u32 u2;
typedef __attribute__((ext_vector_type(4))) u32 u4;
typedef __attribute__((ext_vector_type(2))) __bf16 h2;
typedef __attribute__((ext_vector_type(8))) __bf16 h8;
typedef __amdgpu_buffer_rsrc_t rsrc_t;
#define DI __device__ __forceinline__
#define LDSP(T, off) ((__attribute__((address_space(3))) T*)(lds + (off)))
typedef __attribute__((address_space(3))) char* ldsp_t;

#define STAMP(i) do { __builtin_amdgcn_sched_barrier(0); st[i] = __builtin_amdgcn_s_memrealtime(); __builtin_amdgcn_sched_barrier(0); } while (0)

DI u32 pk(float lo, float hi) { h2 v = {(__bf16)lo, (__bf16)hi}; return __builtin_bit_cast(u32, v); }
DI float asf(u32 x) { return __builtin_bit_cast(float, x); }
DI bool wall(bool p) { return __builtin_amdgcn_ballot_w64(!p) == 0; }
DI int imin(int a,int b){return a<b?a:b;}
DI int imax(int a,int b){return a>b?a:b;}
DI float blo(u32 x) { return asf(x << 16); }
DI float bhi(u32 x) { return asf(x & 0xffff0000u); }
DI float vsin(float r) { return __builtin_amdgcn_sinf(r); }
DI float vcos(float r) { return __builtin_amdgcn_cosf(r); }
DI u32 cs(float r) { return pk(vcos(r), vsin(r)); }
DI f16v mm32(u4 a, u4 b, f16v c) { return __builtin_amdgcn_mfma_f32_32x32x16_bf16(__builtin_bit_cast(h8, a), __builtin_bit_cast(h8, b), c, 0, 0, 0); }
DI f4 mm16(u4 a, u4 b, f4 c) { return __builtin_amdgcn_mfma_f32_16x16x32_bf16(__builtin_bit_cast(h8, a), __builtin_bit_cast(h8, b), c, 0, 0, 0); }
DI void bar() { asm volatile("s_waitcnt lgkmcnt(0)\n\ts_barrier" ::: "memory"); }
DI void drain() { asm volatile("s_waitcnt vmcnt(0)" ::: "memory"); }
DI rsrc_t mkr(const void* p, u32 n) { return __builtin_amdgcn_make_buffer_rsrc((void*)p, 0, n, 0x00020000); }
DI float swp(float v) { return __builtin_bit_cast(float, __builtin_amdgcn_mov_dpp(__builtin_bit_cast(int, v), 0xB1, 0xf, 0xf, true)); }
#define PIN(x) asm volatile("" : "+v"(x))

constexpr u32 SPINCAP = 1u << 16;
constexpr u32 T_OFF = 1u << 16;
constexpr u32 T_P = 129 * 128;
constexpr u32 FLAG_OFF = 4096;

struct Job { u4 c, cp; float ar[4], ai[4], br[4], bi[4]; };

DI void job_mats(Job& J, int g1, int n4, int g) {
  const float s1 = (float)(g1 + 18 * n4) * (1.f / 288.f);
#pragma unroll
  for (int i = 0; i < 4; i++) {
    J.c[i] = cs((float)(4 * g + i) * s1);
    J.cp[i] = cs((float)(n4 * (g1 + 18 * (4 * g + i))) * (1.f / 288.f));
  }
}
DI void job_coef(Job& J, ldsp_t lds, int g1, int g, int sel, int wo, int wp) {
  const float nrm = 0.5f / (2048.f * 288.f);
  const u32 sg = sel ? 0x80000000u : 0u;
#pragma unroll
  for (int r = 0; r < 4; r++) {
    const int gg = g1 + 18 * (4 * g + r);
    const int col = sel ? 576 - 2 * gg : 2 * gg;
    float o_r = blo(*LDSP(u16, wo + col));
    float o_i = asf(((u32)*LDSP(u16, wo + col + 16 * 608) << 16) ^ sg);
    if (r == 0) {
      const float a_r = blo(*LDSP(u16, wp + col));
      const float a_i = asf(((u32)*LDSP(u16, wp + col + 16 * 608) << 16) ^ sg ^ 0x80000000u);
      if (gg == 0) { o_r = 0.5f * (o_r + a_r); o_i = 0.5f * (o_i + a_i); }
    }
    const float p_r = swp(o_r), p_i = swp(o_i);
    const float th = (float)gg * (1.f / 576.f);
    const float s = vsin(th) * nrm, c = vcos(th) * nrm;
    const float d_r = o_r - p_r, d_i = o_i - p_i;
    J.ar[r] = (o_r + p_r) * nrm - s * d_r;
    J.ai[r] = (o_i + p_i) * nrm - s * d_i;
    J.br[r] = -c * d_i;
    J.bi[r] = c * d_r;
  }
}
DI u4 job_run(const Job& J, u4 a, u4 bre, u4 bim, u4 bp) {
  const f4 z = {0.f, 0.f, 0.f, 0.f};
  f4 re = mm16(a, bre, z), im = mm16(a, bim, z);
  u4 d0, d1;
#pragma unroll
  for (int r = 0; r < 4; r++) { d0[r] = pk(re[r], im[r]); d1[r] = pk(im[r], -re[r]); }
  re = mm16(J.c, d0, z); im = mm16(J.c, d1, z);
#pragma unroll
  for (int r = 0; r < 4; r++) {
    const float pr = swp(re[r]), pi = swp(im[r]);
    const float yr = J.ar[r] * re[r] - J.ai[r] * im[r] + J.br[r] * pr - J.bi[r] * pi;
    const float yi = J.ar[r] * im[r] + J.ai[r] * re[r] + J.br[r] * pi + J.bi[r] * pr;
    d0[r] = pk(yi, yr); d1[r] = pk(yr, -yi);
  }
  re = mm16(d0, J.cp, z); im = mm16(d1, J.cp, z);
#pragma unroll
  for (int r = 0; r < 4; r++) { d0[r] = pk(re[r], im[r]); d1[r] = pk(im[r], -re[r]); }
  re = mm16(bp, d0, z); im = mm16(bp, d1, z);
#pragma unroll
  for (int r = 0; r < 4; r++) d0[r] = pk(re[r], im[r]);
  return d0;
}

constexpr int B_XB = 0, B_MA = 23040, B_MB = B_MA + 6144, B_WB = B_MB + 4096, B_J3 = B_WB + 19456, B_P3 = B_WB;

DI void role_b(ldsp_t lds, int b, const u16* x, const u16* wr, const u16* wi, u16* out, char* ws, u64* st) {
  const int tid = __builtin_amdgcn_workitem_id_x(), l = tid & 63, w = __builtin_amdgcn_readfirstlane(tid >> 6);
  const int n5 = l & 31, h = l >> 5, n4 = l & 15, g = l >> 4, jj = g & 1;
  const int s2 = b & 7, c = b >> 3, p = s2 * 18 + c;
  const f16v z16 = {0.f, 0.f, 0.f, 0.f, 0.f, 0.f, 0.f, 0.f, 0.f, 0.f, 0.f, 0.f, 0.f, 0.f, 0.f, 0.f};
  u4 xv[2];
  {
    const rsrc_t rx = mkr(x, 2048 * 1152);
    const u32 vo = h * 4 * 147456 + jj * 9216 + n4 * 4, so = s2 * 1152 + c * 64 + w * 18432;
#pragma unroll
    for (int i = 0; i < 8; i++) xv[i >> 2][i & 3] = __builtin_amdgcn_raw_buffer_load_b32(rx, vo, so + ((i >> 2) * 8 + (i & 3)) * 147456, 0);
  }
  const rsrc_t rw = mkr(ws, 4u << 20);
  u32 tag = __builtin_amdgcn_raw_buffer_load_b32(rw, 0, 0, 16);
  const int kap = b - 15;
  const int k1 = kap < 112 ? (kap >> 4) + 1 + 16 * (kap & 15) : (kap < 120 ? 8 + 16 * (kap - 112) : 16 * (kap - 120));
  u4 wv[4];
  if (kap >= 0) {
    const rsrc_t r0 = mkr(wr, 2048 * 578), r1 = mkr(wi, 2048 * 578);
    const int ka_ = k1 + 256 * w, kb_ = (2048 - ka_) & 2047;
    const u32 vo = l < 37 ? l * 16 : 0x7ffffff0u;
    wv[0] = __builtin_amdgcn_raw_buffer_load_b128(r0, vo, (ka_ * 578) & ~15, 0);
    wv[1] = __builtin_amdgcn_raw_buffer_load_b128(r0, vo, (kb_ * 578) & ~15, 0);
    wv[2] = __builtin_amdgcn_raw_buffer_load_b128(r1, vo, (ka_ * 578) & ~15, 0);
    wv[3] = __builtin_amdgcn_raw_buffer_load_b128(r1, vo, (kb_ * 578) & ~15, 0);
  }
  __builtin_amdgcn_sched_barrier(0);
  const int comp = n5 & 1, idx = 2 * (((n5 >> 1) & 1) + 2 * (n5 >> 3)) + ((n5 >> 2) & 1);
  const float q4 = comp ? 0.25f : 0.f;
  u4 m1[2], m2[2];
  const int kaw = w ? w : 8;
  {
    const float a1 = (float)idx * (1.f / 16.f), o1 = idx >= 9 ? -q4 : q4, a2 = (float)(kaw + 16 * idx) * (1.f / 256.f);
#pragma unroll
    for (int i = 0; i < 8; i++) {
      const float j = (float)((i >> 2) * 8 + (i & 3) + 4 * h);
      m1[i >> 2][i & 3] = cs(j * a1 + o1);
      m2[i >> 2][i & 3] = cs(j * a2 + q4);
    }
  }
  Job J0, J1;
  if (kap >= 0) {
    u4 f;
    if (w < 6) {
      const int T = w >= 3, t = w - 3 * T;
      const int g1 = idx + 16 * T;
#pragma unroll
      for (int i = 0; i < 4; i++) { const int ii = 4 * t + i; f[i] = ii <= 8 ? cs((float)((2 * ii + h) * g1) * (1.f / 18.f) + q4) : 0u; }
      *LDSP(u4, B_MA + w * 1024 + l * 16) = f;
    } else {
      u4 f2_;
#pragma unroll
      for (int i = 0; i < 2; i++) {
        const int u = 2 * g + i, v = n4 >> 1;
        const int e = w == 6 ? u * (k1 + 256 * v) : v * (k1 + 256 * u);
        const float a = (float)(e & 2047) * (1.f / 2048.f), cc = vcos(a), ss = vsin(a);
        const u32 re = pk(cc, ss), im = pk(-ss, cc);
        f[2 * i] = (n4 & 1) ? 0u : re; f[2 * i + 1] = (n4 & 1) ? re : 0u;
        f2_[2 * i] = (n4 & 1) ? 0u : im; f2_[2 * i + 1] = (n4 & 1) ? im : 0u;
      }
      *LDSP(u4, B_MB + (w - 6) * 2048 + l * 16) = f;
      *LDSP(u4, B_MB + (w - 6) * 2048 + 1024 + l * 16) = f2_;
    }
  }
  PIN(m1[0]); PIN(m1[1]); PIN(m2[0]); PIN(m2[1]);
  STAMP(1);
  const int wadr = B_XB + (h * 16 + n4) * 80 + (2 * w + jj) * 4;
  {
    f16v a = mm32(m1[0], xv[0], z16);
    a = mm32(m1[1], xv[1], a);
    STAMP(2);
#pragma unroll
    for (int q = 0; q < 8; q++) *LDSP(u32, wadr + q * 2560) = pk(a[2 * q], a[2 * q + 1]);
  }
  bar();
  {
    const int kr = jj ? 16 - kaw : kaw;
    const u32 sg = (w == 0 && jj) ? 0x80000000u : 0u;
    u4 d0 = *LDSP(u4, B_XB + (kr * 16 + n4) * 80 + h * 16), d1 = *LDSP(u4, B_XB + (kr * 16 + n4) * 80 + h * 16 + 32);
    const u32 so = T_OFF + p * T_P + (kaw - 1) * 2048;
    if (w == 0) {
      d0 ^= sg; d1 ^= sg;
      u4 e0 = *LDSP(u4, B_XB + n4 * 80 + h * 16), e1 = *LDSP(u4, B_XB + n4 * 80 + h * 16 + 32);
      e0 ^= sg; e1 ^= sg;
      f16v a = mm32(m2[0], d0, z16), a0 = mm32(m1[0], e0, z16);
      a = mm32(m2[1], d1, a); a0 = mm32(m1[1], e1, a0);
#pragma unroll
      for (int q = 0; q < 4; q++) __builtin_amdgcn_raw_buffer_store_b32(pk(a[2 * q], a[2 * q + 1]) & ~1u, rw, l * 4 + q * 256, so, 16);
#pragma unroll
      for (int q = 0; q < 4; q++) __builtin_amdgcn_raw_buffer_store_b32(pk(a0[2 * q], a0[2 * q + 1]) & ~1u, rw, l * 4 + q * 256 + 1024, so, 16);
      __builtin_amdgcn_raw_buffer_store_b32(pk(a0[8], a0[9]) & ~1u, rw, h ? 0x7ffffff0u : l * 4 + 2048, so, 16);
    } else {
      f16v a = mm32(m2[0], d0, z16);
      a = mm32(m2[1], d1, a);
#pragma unroll
      for (int q = 0; q < 8; q++) __builtin_amdgcn_raw_buffer_store_b32(pk(a[2 * q], a[2 * q + 1]) & ~1u, rw, l * 4 + q * 256, so, 16);
    }
  }
  STAMP(3);
  __builtin_amdgcn_sched_barrier(0);
  tag = tag * 1664525u + 1013904223u;
  if (kap >= 0) {
    if (l < 37) {
#pragma unroll
      for (int i = 0; i < 4; i++) *LDSP(u4, B_WB + ((i >> 1) * 16 + 2 * w + (i & 1)) * 608 + l * 16) = wv[i];
    }
    job_mats(J0, w, n4, g);
    job_mats(J1, w + 8, n4, g);
    PIN(J0.c); PIN(J0.cp); PIN(J1.c); PIN(J1.cp);
  }
  drain();
  bar();
  if (tid == 0) __builtin_amdgcn_raw_buffer_store_b32(tag, rw, 0, FLAG_OFF + p * 128, 16);
  STAMP(4);
  if (kap >= 0) {
    {
      const int sel = n4 & 1, kk = k1 + 256 * (n4 >> 1), kn = (2048 - kk) & 2047;
      const int wo = B_WB + n4 * 608 + (((sel ? kn : kk) * 578) & 15), wp = B_WB + (n4 ^ 1) * 608 + (((sel ? kk : kn) * 578) & 15);
      job_coef(J0, lds, w, g, sel, wo, wp);
      job_coef(J1, lds, w + 8, g, sel, wo, wp);
      if (w >= 6) {
        Job J2;
        job_mats(J2, w + 10, n4, g);
        job_coef(J2, lds, w + 10, g, sel, wo, wp);
        const int o = B_J3 + (w - 6) * 6144 + l * 16;
        *LDSP(u4, o) = J2.c; *LDSP(u4, o + 1024) = J2.cp;
        *LDSP(f4, o + 2048) = (f4){J2.ar[0], J2.ar[1], J2.ar[2], J2.ar[3]};
        *LDSP(f4, o + 3072) = (f4){J2.ai[0], J2.ai[1], J2.ai[2], J2.ai[3]};
        *LDSP(f4, o + 4096) = (f4){J2.br[0], J2.br[1], J2.br[2], J2.br[3]};
        *LDSP(f4, o + 5120) = (f4){J2.bi[0], J2.bi[1], J2.bi[2], J2.bi[3]};
      }
#pragma unroll
      for (int r = 0; r < 4; r++) { PIN(J0.ar[r]); PIN(J0.ai[r]); PIN(J0.br[r]); PIN(J0.bi[r]); PIN(J1.ar[r]); PIN(J1.ai[r]); PIN(J1.br[r]); PIN(J1.bi[r]); }
    }
    STAMP(5);
    _Pragma("nounroll") for (u32 sp = 0; sp < SPINCAP; sp++) {
      const u32 f = __builtin_amdgcn_raw_buffer_load_b32(rw, (l < 18 ? l : 0) * 128, FLAG_OFF + w * 18 * 128, 16);
      if (wall(f == tag)) break;
      asm volatile("" ::: "memory");
    }
    asm volatile("" ::: "memory");
    STAMP(6);
    const u32 vo = h * T_P + n5 * 4, so = T_OFF + w * 18 * T_P + kap * 128;
    {
      u4 v0, v1, v2 = {0u, 0u, 0u, 0u};
#pragma unroll
      for (int i = 0; i < 4; i++) v0[i] = __builtin_amdgcn_raw_buffer_load_b32(rw, vo, so + 2 * i * T_P, 16);
#pragma unroll
      for (int i = 0; i < 4; i++) v1[i] = __builtin_amdgcn_raw_buffer_load_b32(rw, vo, so + 2 * (i + 4) * T_P, 16);
      v2[0] = __builtin_amdgcn_raw_buffer_load_b32(rw, vo, so + 16 * T_P, 16);
      f16v a = mm32(*LDSP(u4, B_MA + l * 16), v0, z16), y = mm32(*LDSP(u4, B_MA + 3072 + l * 16), v0, z16);
      a = mm32(*LDSP(u4, B_MA + 1024 + l * 16), v1, a); y = mm32(*LDSP(u4, B_MA + 4096 + l * 16), v1, y);
      a = mm32(*LDSP(u4, B_MA + 2048 + l * 16), v2, a); y = mm32(*LDSP(u4, B_MA + 5120 + l * 16), v2, y);
      STAMP(7);
#pragma unroll
      for (int q = 0; q < 8; q++) *LDSP(u32, wadr + q * 2560) = pk(a[2 * q], a[2 * q + 1]);
      *LDSP(u32, wadr + 8 * 2560) = pk(y[0], y[1]);
    }
    const u4 bre = *LDSP(u4, B_MB + l * 16), bim = *LDSP(u4, B_MB + 1024 + l * 16), bp = *LDSP(u4, B_MB + 2048 + l * 16);
    bar();
    STAMP(8);
    if (kap == 0 && tid == 0) __builtin_amdgcn_raw_buffer_store_b32(tag, rw, 0, 0, 16);
    {
      const int ja = B_XB + (w * 16 + n4) * 80 + g * 16;
      const u4 a0 = *LDSP(u4, ja), a1 = *LDSP(u4, ja + 8 * 1280);
      const u4 r0 = job_run(J0, a0, bre, bim, bp), r1 = job_run(J1, a1, bre, bim, bp);
      *LDSP(u4, ja) = r0; *LDSP(u4, ja + 8 * 1280) = r1;
      if (w >= 6) {
        Job J2;
        const int o = B_J3 + (w - 6) * 6144 + l * 16;
        J2.c = *LDSP(u4, o); J2.cp = *LDSP(u4, o + 1024);
        const f4 t0 = *LDSP(f4, o + 2048), t1 = *LDSP(f4, o + 3072), t2 = *LDSP(f4, o + 4096), t3 = *LDSP(f4, o + 5120);
#pragma unroll
        for (int r = 0; r < 4; r++) { J2.ar[r] = t0[r]; J2.ai[r] = t1[r]; J2.br[r] = t2[r]; J2.bi[r] = t3[r]; }
        *LDSP(u4, ja + 10 * 1280) = job_run(J2, *LDSP(u4, ja + 10 * 1280), bre, bim, bp);
      }
    }
    bar();
    STAMP(9);
    {
      u4 v0, v1, v2 = {0u, 0u, 0u, 0u};
#pragma unroll
      for (int i = 0; i < 4; i++) { v0[i] = *LDSP(u32, wadr + i * 2560); v1[i] = *LDSP(u32, wadr + (i + 4) * 2560); }
      v2[0] = *LDSP(u32, wadr + 8 * 2560);
      f16v a = mm32(*LDSP(u4, B_MA + l * 16), v0, z16), y = mm32(*LDSP(u4, B_MA + 3072 + l * 16), v0, z16);
      a = mm32(*LDSP(u4, B_MA + 1024 + l * 16), v1, a); y = mm32(*LDSP(u4, B_MA + 4096 + l * 16), v1, y);
      a = mm32(*LDSP(u4, B_MA + 2048 + l * 16), v2, a); y = mm32(*LDSP(u4, B_MA + 5120 + l * 16), v2, y);
#pragma unroll
      for (int q = 0; q < 8; q++) __builtin_amdgcn_raw_buffer_store_b32(pk(a[2 * q], a[2 * q + 1]) | 1u, rw, vo, so + 2 * q * T_P, 16);
      __builtin_amdgcn_raw_buffer_store_b32(pk(y[0], y[1]) | 1u, rw, vo, so + 16 * T_P, 16);
    }
    STAMP(10);
  }
  __builtin_amdgcn_sched_barrier(0);
  u4 m3[2], m30[2], m4[2];
  u32 la[8];
#pragma unroll
  for (int i = 0; i < 8; i++) {
    const int kb = 2 * i + h;
    float a = (float)(idx * (kaw + 16 * kb)) * (1.f / 256.f) + q4;
    if (w == 0 && i >= 4) a = 0.5f - a;
    m3[i >> 2][i & 3] = cs(a);
    const int ka = (i >> 2) * 8 + (i & 3) + 4 * h;
    float a4 = (float)(idx * ka) * (1.f / 16.f) + q4;
    if (ka >= 9) a4 = 0.5f - a4;
    m4[i >> 2][i & 3] = cs(a4);
    la[i] = l * 4 + i * 256;
  }
  if (w == 0) {
#pragma unroll
    for (int i = 0; i < 8; i++) {
      const int kb = 2 * i + h;
      float a = (float)(idx * kb) * (1.f / 16.f) + q4;
      if (kb >= 9) a = 0.5f - a;
      m30[i >> 2][i & 3] = cs(a);
      const int ln = jj ? (kb <= 8 ? 8 + kb : 24 - kb) : (kb <= 7 ? kb : 15 - kb);
      la[i] = ln * 128 + (kb > 7 + jj ? 64 : 0) + n4 * 4;
    }
  }
  PIN(m3[0]); PIN(m3[1]); PIN(m4[0]); PIN(m4[1]);
  drain();
  STAMP(11);
  {
    const u32 so = T_OFF + p * T_P + (kaw - 1) * 2048;
    u4 v0, v1;
    _Pragma("nounroll") for (u32 sp = 0; sp < SPINCAP; sp++) {
#pragma unroll
      for (int i = 0; i < 4; i++) v0[i] = __builtin_amdgcn_raw_buffer_load_b32(rw, la[i], so, 16);
#pragma unroll
      for (int i = 0; i < 4; i++) v1[i] = __builtin_amdgcn_raw_buffer_load_b32(rw, la[i + 4], so, 16);
      const u4 m = v0 & v1;
      if (wall(m[0] & m[1] & m[2] & m[3] & 1u)) break;
      if (kap < 0) __builtin_amdgcn_s_sleep(4);
      asm volatile("" ::: "memory");
    }
    STAMP(12);
    f16v a;
    int kr = jj ? 16 - kaw : kaw;
    if (w == 0) {
      const u32 mk = jj ? 0u : ~0u;
      a = mm32(m3[0], v0 & mk, z16);
      a = mm32(m3[1], v1 & mk, a);
      a = mm32(m30[0], v0 & ~mk, a);
      a = mm32(m30[1], v1 & ~mk, a);
      kr = jj ? 0 : 8;
    } else {
      a = mm32(m3[0], v0, z16);
      a = mm32(m3[1], v1, a);
    }
    const int wa = B_P3 + (h * 16 + n4) * 80 + kr * 4;
#pragma unroll
    for (int q = 0; q < 8; q++) *LDSP(u32, wa + q * 2560) = pk(a[2 * q], a[2 * q + 1]);
  }
  bar();
  STAMP(13);
  {
    const int ra = B_P3 + ((2 * w + jj) * 16 + n4) * 80 + h * 16;
    f16v a = mm32(m4[0], *LDSP(u4, ra), z16);
    a = mm32(m4[1], *LDSP(u4, ra + 32), a);
    const rsrc_t ro = mkr(out, 2048 * 2304);
    const u32 vo = h * 294912 + jj * 18432 + n4 * 4, so = s2 * 2304 + 1152 + c * 64 + w * 36864;
#pragma unroll
    for (int q = 0; q < 8; q++) __builtin_amdgcn_raw_buffer_store_b32(pk(a[2 * q + 1], a[2 * q]), ro, vo, so + q * 589824, 17);
  }
  STAMP(14);
}

constexpr int A_PB = 8704, A_TAB = 8 * A_PB, A_TS = 3072;
constexpr int A_U = 3072 + 512;
constexpr int TE = 8, TO = TE + 303, T2A = 608, T2B = 656, T3 = 704;

struct cf { float r, i; };
DI cf cmul(cf a, cf b) { return {a.r * b.r - a.i * b.i, a.r * b.i + a.i * b.r}; }
DI cf cexpo(float lr, float rev) { const float m = __builtin_amdgcn_exp2f(lr), f = __builtin_amdgcn_fractf(rev); return {m * vcos(f), m * vsin(f)}; }
DI u4 boxf(int n4, int g, int off, int wd) {
  const u32 m = (((1u << (2 * wd + 1)) - 1u) << 20) >> imin(imax(20 - (16 + n4 - 8 * g - off - wd), 0), 31);
  u4 r;
#pragma unroll
  for (int i = 0; i < 4; i++) r[i] = ((((m >> (2 * i)) & 3u) * 0x8001u) & 0x10001u) * 0x3f80u;
  return r;
}
DI u4 rd4(ldsp_t lds, int a) {
  return (u4){*LDSP(u32, a), *LDSP(u32, a - 4), *LDSP(u32, a - 8), *LDSP(u32, a - 12)};
}
DI u4 pk4(f4 a, f4 b) { return (u4){pk(a[0], b[0]), pk(a[1], b[1]), pk(a[2], b[2]), pk(a[3], b[3])}; }

template <int I0, int I1>
DI void conv(ldsp_t lds, int ua, int fa, int ta, const u4& b3, const u4& b5, const u4& b7, const u4& b9, int ya) {
  const f4 z = {0.f, 0.f, 0.f, 0.f};
  f4 acc[I1 - I0];
#pragma unroll
  for (int i = 0; i < I1 - I0; i++) acc[i] = z;
  constexpr int U0 = I0 > 0 ? I0 - 1 : 0;
#pragma unroll 2
  for (int s = 0; s < 8; s++) {
    const u4 f0 = rd4(lds, fa + s * 64), f1 = rd4(lds, fa + s * 64 + 512);
#pragma unroll
    for (int i = U0; i < I1; i++) {
      const u4 u = *LDSP(u4, ua + i * 512 - s * 64);
      if (i >= I0) acc[i - I0] = mm16(f0, u, acc[i - I0]);
      if (i + 1 < I1) acc[i + 1 - I0] = mm16(f1, u, acc[i + 1 - I0]);
    }
  }
  const u4 f16_ = rd4(lds, fa + 16 * 64), f17 = rd4(lds, fa + 17 * 64);
#pragma unroll
  for (int j = 0; j < 6; j++) {
    const bool s2 = (j + 2 < I1) && (j + 4 >= I0), s3 = (j + 4 < I1);
    if (!s2 && !s3) continue;
    const u4 x = *LDSP(u4, ua + j * 512), y = *LDSP(u4, ua + j * 512 - 16);
    if (j + 2 >= I0 && j + 2 < I1) acc[j + 2 - I0] = mm16(f16_, x, acc[j + 2 - I0]);
    if (j + 4 >= I0 && j + 4 < I1) acc[j + 4 - I0] = mm16(f17, x, acc[j + 4 - I0]);
    if (s2) {
      const u4 pa = pk4(mm16(x, b3, z), mm16(x, b5, z)), pb = pk4(mm16(y, b3, z), mm16(y, b5, z));
#pragma unroll
      for (int d = 0; d < 3; d++) {
        const int i = j + 2 + d;
        if (i >= I0 && i < I1) {
          acc[i - I0] = mm16(pa, rd4(lds, ta + T2A * 4 + d * 64), acc[i - I0]);
          acc[i - I0] = mm16(pb, rd4(lds, ta + T2B * 4 + d * 64), acc[i - I0]);
        }
      }
    }
    if (s3) {
      const u4 pc = pk4(mm16(x, b7, z), mm16(y, b9, z));
#pragma unroll
      for (int d = 0; d < 4; d++) {
        const int i = j + 4 + d;
        if (i >= I0 && i < I1) acc[i - I0] = mm16(pc, rd4(lds, ta + T3 * 4 + d * 64), acc[i - I0]);
      }
    }
  }
#pragma unroll
  for (int i = 0; i < I1 - I0; i++) *LDSP(u2, ya + i * 512) = (u2){pk(acc[i][0], acc[i][1]), pk(acc[i][2], acc[i][3])};
}

DI void role_a(ldsp_t lds, int a, const u16* u, const u16* Are, const u16* Aim, const u16* Bre, const u16* Bim, const u16* Cre,
               const u16* Cim, const u16* ldt, const u16* dsk, u16* out, u64* st) {
  const int tid = __builtin_amdgcn_workitem_id_x(), l = tid & 63, w = __builtin_amdgcn_readfirstlane(tid >> 6);
  const int n5 = l & 31, h = l >> 5, n4 = l & 15, g = l >> 4;
  const int xc = a & 7, i0 = a >> 3, grp = i0 < 16 ? xc * 16 + i0 : 128 + 2 * xc + (i0 - 16);
  const int ch = w & 3, hf = w >> 2, d = grp * 4 + ch;
  const int pi_ = d * 128 + hf * 64 + l;
  const u32 vare = Are[pi_], vaim = Aim[pi_], vbre = Bre[pi_], vbim = Bim[pi_], vcre = Cre[pi_], vcim = Cim[pi_], vldt = ldt[d];
  const int t4 = (tid + i0 * 28) & 511;
  u2 uv[4];
  {
    const rsrc_t ru = mkr(u, 2048 * 1152);
#pragma unroll
    for (int i = 0; i < 4; i++) uv[i] = __builtin_amdgcn_raw_buffer_load_b64(ru, t4 * 4608 + i * 1152, grp * 8, 0);
  }
  const u2 dv = *(const u2*)(dsk + grp * 4);
  __builtin_amdgcn_sched_barrier(0);
  if (tid < 384) { *LDSP(u4, A_TAB + tid * 32) = (u4){0u, 0u, 0u, 0u}; *LDSP(u4, A_TAB + tid * 32 + 16) = (u4){0u, 0u, 0u, 0u}; }
  u4 b3 = boxf(n4, g, 4, 1), b5 = boxf(n4, g, 8, 2), b7 = boxf(n4, g, 8, 3), b9 = boxf(n4, g, 8, 4);
  PIN(b3); PIN(b5); PIN(b7); PIN(b9);
  STAMP(1);
  const int pb = w * A_PB;
  f16v kacc;
  {
    const float dt = blo(pk(expf(blo(vldt)), 0.f));
    const float lr = dt * blo(vare) * 1.44269504088896341f, rv = dt * blo(vaim) * 0.15915494309189535f;
    const cf c0 = cmul((cf){blo(vcre), blo(vcim)}, (cf){dt * blo(vbre), dt * blo(vbim)});
    STAMP(2);
    const cf w1 = cexpo(lr, rv), w8 = cexpo(8.f * lr, 8.f * rv), V = cexpo(256.f * lr, 256.f * rv);
    const int wa = pb + l * 4;
#define PUTG(row, v) *LDSP(u32, wa + (row) * 272) = pk((v).r, -(v).i)
    cf gk = c0, t, t4_, t8;
    PUTG(0, gk); t = cmul(gk, V); PUTG(9, t);
    gk = cmul(gk, w1); PUTG(1, gk);
    gk = cmul(gk, w1); PUTG(2, gk); t = cmul(gk, V); PUTG(10, t);
    gk = cmul(gk, w1); PUTG(3, gk);
    gk = cmul(gk, w1); PUTG(4, gk); t4_ = cmul(gk, V); PUTG(11, t4_);
    gk = cmul(gk, w1); PUTG(5, gk); t4_ = cmul(t4_, V); PUTG(14, t4_);
    gk = cmul(gk, w1); PUTG(6, gk); t = cmul(gk, V); PUTG(12, t);
    gk = cmul(gk, w1); PUTG(7, gk); t4_ = cmul(t4_, V); PUTG(16, t4_);
    gk = cmul(gk, w1); PUTG(8, gk);
    t8 = cmul(gk, V); PUTG(13, t8);
    t8 = cmul(t8, V); PUTG(15, t8);
    t8 = cmul(t8, V); PUTG(17, t8);
    t8 = cmul(t8, V); PUTG(18, t8);
    t8 = cmul(t8, V); PUTG(19, t8);
    t8 = cmul(t8, V); PUTG(20, t8);
    t8 = cmul(t8, V); PUTG(21, t8);
    const int reg = (n5 & 3) + 4 * (n5 >> 3), hh = (n5 >> 2) & 1;
    const int grow = reg >= 12 ? 14 + (reg & 1) + 2 * hh + 2 * (reg & 2) : (reg > 8 ? 0 : (hh ? 9 + (int)((0x443222100ull >> (4 * reg)) & 15) : reg));
    u4 gf[8];
    asm volatile("" ::: "memory");
#pragma unroll
    for (int t_ = 0; t_ < 8; t_++) gf[t_] = *LDSP(u4, pb + grow * 272 + h * 16 + t_ * 32);
    asm volatile("" ::: "memory");
    const cf w16 = cmul(w8, w8);
    cf he = {1.f, 0.f}, ho = w8;
#pragma unroll
    for (int q = 0; q < 32; q += 2) {
      *LDSP(u32, wa + q * 272) = pk(he.r, he.i);
      *LDSP(u32, wa + (q + 1) * 272) = pk(ho.r, ho.i);
      if (q == 0) he = w16; else if (q < 30) he = cmul(he, w16);
      if (q < 30) ho = cmul(ho, w16);
    }
    asm volatile("" ::: "memory");
    STAMP(3);
    kacc = (f16v){0.f, 0.f, 0.f, 0.f, 0.f, 0.f, 0.f, 0.f, 0.f, 0.f, 0.f, 0.f, 0.f, 0.f, 0.f, 0.f};
#pragma unroll
    for (int t_ = 0; t_ < 8; t_++) kacc = mm32(gf[t_], *LDSP(u4, pb + n5 * 272 + h * 16 + t_ * 32), kacc);
    asm volatile("" ::: "memory");
  }
  if (hf) {
    *LDSP(f4, pb + l * 16) = (f4){kacc[0], kacc[1], kacc[2], kacc[3]};
    *LDSP(f4, pb + 1024 + l * 16) = (f4){kacc[4], kacc[5], kacc[6], kacc[7]};
    *LDSP(float, pb + 2048 + l * 4) = kacc[8];
  } else {
    *LDSP(f4, pb + l * 16) = (f4){kacc[12], kacc[13], kacc[14], kacc[15]};
  }
  STAMP(4);
  bar();
  STAMP(5);
  const int tb = A_TAB + ch * A_TS;
  {
    const int po = (w ^ 4) * A_PB;
    if (hf) {
      const f4 v = *LDSP(f4, po + l * 16);
      const u32 p0 = pk(kacc[12] + v[0], kacc[13] + v[1]), p1 = pk(kacc[14] + v[2], kacc[15] + v[3]);
      *LDSP(u32, tb + (((n5 & 1) ? T2B : T2A) + 16 * h + (n5 >> 1)) * 4) = p0;
      *LDSP(u16, tb + T3 * 4 + (64 * h + n5) * 2) = (u16)p1;
      *LDSP(u16, tb + T3 * 4 + (64 * h + 32 + n5) * 2) = (u16)(p1 >> 16);
      if (l == 63) {
        *LDSP(u32, tb + (TE + 273) * 4) = p0 >> 16;
        *LDSP(u32, tb + (TE + 274) * 4) = p0 & 0xffff0000u;
        *LDSP(u32, tb + (TO + 274) * 4) = (p0 >> 16) | (p0 & 0xffff0000u);
      }
      if (l < 32) *LDSP(u4, ch * A_PB + A_U - 512 + l * 16) = (u4){0u, 0u, 0u, 0u};
    } else {
      const f4 v0 = *LDSP(f4, po + l * 16), v1 = *LDSP(f4, po + 1024 + l * 16);
      const float v2 = *LDSP(float, po + 2048 + l * 4);
      float k[9];
#pragma unroll
      for (int i = 0; i < 4; i++) { k[i] = kacc[i] + v0[i]; k[i + 4] = kacc[i + 4] + v1[i]; }
      k[8] = kacc[8] + v2;
      const u4 e = {pk(k[1], k[0]), pk(k[3], k[2]), pk(k[5], k[4]), pk(k[7], k[6])};
      const u4 o = {pk(k[2], k[1]), pk(k[4], k[3]), pk(k[6], k[5]), pk(k[8], k[7])};
      *LDSP(u4, tb + TE * 4 + l * 16) = e;
      *LDSP(u4, tb + (TO + 1) * 4 + l * 16) = o;
      if (l == 0) *LDSP(u32, tb + TO * 4) = e[0] >> 16;
      if (l == 63) {
        *LDSP(u32, tb + (TE + 256) * 4) = o[3];
        *LDSP(u32, tb + (TE + 257) * 4) = o[3] & 0xffff0000u;
        *LDSP(u32, tb + (TO + 257) * 4) = o[3];
      }
    }
#pragma unroll
    for (int cc = 0; cc < 4; cc++) {
      const u32 sel = (cc & 1) ? 0x07060302u : 0x05040100u;
      *LDSP(u2, cc * A_PB + A_U + t4 * 8) = (u2){__builtin_amdgcn_perm(uv[1][cc >> 1], uv[0][cc >> 1], sel), __builtin_amdgcn_perm(uv[3][cc >> 1], uv[2][cc >> 1], sel)};
    }
  }
  STAMP(6);
  bar();
  STAMP(7);
  {
    const int ua = ch * A_PB + A_U + n4 * 32 + g * 16 - 32;
    const int fa = tb + (((n4 & 1) ? TE : TO) + ((16 + n4 - 8 * g) >> 1)) * 4;
    const int ta = tb + (n4 - 4 * g) * 4;
    const int ya = pb + n4 * 32 + g * 8;
    if (hf) conv<5, 8>(lds, ua, fa, ta, b3, b5, b7, b9, ya);
    else conv<0, 5>(lds, ua, fa, ta, b3, b5, b7, b9, ya);
  }
  STAMP(8);
  bar();
  STAMP(9);
  {
    const int yo = t4 < 320 ? t4 * 8 : 4 * A_PB + (t4 - 320) * 8;
    u2 y[4];
#pragma unroll
    for (int cc = 0; cc < 4; cc++) y[cc] = *LDSP(u2, cc * A_PB + yo);
    const rsrc_t ro = mkr(out, 2048 * 2304);
    const float d0 = blo(dv[0]), d1 = bhi(dv[0]), d2 = blo(dv[1]), d3 = bhi(dv[1]);
#pragma unroll
    for (int i = 0; i < 4; i++) {
      const u32 s0 = pk(blo(uv[i][0]) * d0, bhi(uv[i][0]) * d1), s1 = pk(blo(uv[i][1]) * d2, bhi(uv[i][1]) * d3);
      float yy[4];
#pragma unroll
      for (int cc = 0; cc < 4; cc++) yy[cc] = (i & 1) ? bhi(y[cc][i >> 1]) : blo(y[cc][i >> 1]);
      const u2 o = {pk(yy[0] + blo(s0), yy[1] + bhi(s0)), pk(yy[2] + blo(s1), yy[3] + bhi(s1))};
      __builtin_amdgcn_raw_buffer_store_b64(o, ro, t4 * 9216 + i * 2304, grp * 8, 17);
    }
  }
  STAMP(10);
}

#if defined(__HIP_DEVICE_COMPILE__)
__attribute__((target("no-packed-fp32-ops")))
#endif
__global__ void __launch_bounds__(512, 4) fused_kernel(const u16* __restrict__ u, const u16* __restrict__ Are, const u16* __restrict__ Aim,
    const u16* __restrict__ Bre, const u16* __restrict__ Bim, const u16* __restrict__ Cre, const u16* __restrict__ Cim,
    const u16* __restrict__ ldt, const u16* __restrict__ dsk, const u16* __restrict__ x, const u16* __restrict__ wr,
    const u16* __restrict__ wi, u16* out, char* ws) {
  __shared__ __attribute__((aligned(16))) char smem[81920];
  ldsp_t lds = (ldsp_t)smem;
  const int b = __builtin_amdgcn_workgroup_id_x();
  u64 st[16];
#pragma unroll
  for (int i = 0; i < 16; i++) st[i] = 0;
  STAMP(0);
  if (b < 144) role_b(lds, b, x, wr, wi, out, ws, st);
  else role_a(lds, b - 144, u, Are, Aim, Bre, Bim, Cre, Cim, ldt, dsk, out, st);
  drain();
  STAMP(15);
  if (b == 40 && __builtin_amdgcn_readfirstlane(__builtin_amdgcn_workitem_id_x() >> 6) == 1) { const u64 until = st[0] + 4000 + 10 * (st[0] - st[0]);
    _Pragma("nounroll") for (int q = 0; q < 100000; q++) { if (__builtin_amdgcn_s_memrealtime() >= until) break; } }
}

extern "C" void kernel_launch(void* const* d_in, const int* in_sizes, int n_in, void* d_out, int out_size, void* d_ws, size_t ws_size,
                              hipStream_t stream) {
  const u16* const* in = (const u16* const*)d_in;
  fused_kernel<<<288, 512, 0, stream>>>(in[0], in[1], in[2], in[3], in[4], in[5], in[6], in[7], in[8], in[9], in[10], in[11],
                                        (u16*)d_out, (char*)d_ws);
}
```

```cpp
#include <hip/hip_runtime.h>

typedef unsigned u32;

__global__ void __launch_bounds__(768, 3) fused_kernel(
    const void* x, u32 x_n, u32 x_f, const void* wr, u32 wr_n, u32 wr_f, const void* wi, u32 wi_n, u32 wi_f,
    void* ws, u32 ws_n, u32 ws_f, void* out, u32 out_n, u32 out_f, const void* u, u32 u_n, u32 u_f,
    const void* a_re, const void* a_im, const void* b_re, const void* b_im, const void* c_re, const void* c_im,
    const void* log_dt, const void* dskip) {
#if defined(__HIP_DEVICE_COMPILE__)
  __shared__ __attribute__((aligned(16))) char smem[149376];
  register u32 tid asm("v0") = __builtin_amdgcn_workitem_id_x();
  asm volatile(
    ".p2align 3\n"
    ".Lkstart:\n"
    "s_load_dwordx16 s[28:43], %0, 96\n"
    "s_load_dwordx16 s[4:19], %0, 0\n"
    "s_load_dwordx8 s[20:27], %0, 64\n"
    "v_lshrrev_b32 v165, 6, v0\n"
    "s_mov_b32 s45, %1\n"
    "v_readfirstlane_b32 s44, v165\n"
    "s_cmp_ge_u32 s45, 8\n"
    "s_cbranch_scc1 .Lk1\n"
    "s_getpc_b64 s[46:47]\n"
    ".Lkpc:\n"
    "v_lshlrev_b32 v165, 7, v0\n"
    "v_cmp_gt_u32 vcc, .Lklast-.Lkpc, v165\n"
    "s_and_b64 exec, exec, vcc\n"
    "s_cbranch_execz .Lk2\n"
    "global_load_dword v1, v165, s[46:47]\n"
    "s_waitcnt vmcnt(0)\n"
    ".Lk2:\n"
    "s_mov_b64 exec, -1\n"
    "s_bitcmp1_b32 s44, 2\n"
    "s_cbranch_scc0 .Lk3\n"
    "v_and_b32 v165, 255, v0\n"
    "v_lshl_add_u32 v165, s45, 8, v165\n"
    "v_mul_u32_u24 v165, 1152, v165\n"
    "s_waitcnt lgkmcnt(0)\n"
    "buffer_load_dword v1, v165, s[24:27], 0 offen offset:1024\n"
    "s_waitcnt vmcnt(0)\n"
    ".Lk3:\n"
    "s_endpgm\n"
    ".Lk1:\n"
    "v_and_b32 v1, 63, v0\n"
    "v_and_b32 v167, 15, v0\n"
    "v_bfe_u32 v166, v0, 4, 2\n"
    "s_sub_u32 s45, s45, 112\n"
    "s_cmp_ge_u32 s45, 144\n"
    "s_cbranch_scc1 .LkA\n"
    "s_setprio 1\n"
    "s_memrealtime s[46:47]\n"
    "s_and_b32 s48, s45, 7\n"
    "s_lshr_b32 s49, s45, 3\n"
    "v_bfe_u32 v165, v0, 5, 1\n"
    "v_bfe_u32 v164, v0, 4, 1\n"
    "s_mul_i32 s54, s48, 18\n"
    "s_add_u32 s54, s54, s49\n"
    "s_mul_i32 s52, s54, 17792\n"
    "s_cmp_ge_u32 s44, 8\n"
    "s_cbranch_scc1 .Lk4\n"
    "v_mul_u32_u24 v162, 589824, v165\n"
    "v_mul_u32_u24 v163, 9216, v164\n"
    "s_mul_i32 s57, s48, 1152\n"
    "s_lshl_b32 s55, s49, 6\n"
    "v_lshl_add_u32 v162, v167, 2, v162\n"
    "s_add_u32 s57, s57, s55\n"
    "s_mul_i32 s55, s44, 18432\n"
    "v_add_u32 v162, v162, v163\n"
    "s_add_u32 s57, s57, s55\n"
    "s_add_u32 s58, s57, 147456\n"
    "s_add_u32 s59, s57, 294912\n"
    "s_add_u32 s60, s57, 442368\n"
    "s_add_u32 s61, s57, 1179648\n"
    "s_add_u32 s62, s57, 1327104\n"
    "s_add_u32 s63, s57, 1474560\n"
    "s_add_u32 s64, s57, 1622016\n"
    "s_waitcnt lgkmcnt(0)\n"
    "s_nop 0\n"
    "buffer_load_dword v2, v162, s[4:7], s57 offen \n"
    "buffer_load_dword v3, v162, s[4:7], s58 offen \n"
    "buffer_load_dword v4, v162, s[4:7], s59 offen \n"
    "buffer_load_dword v5, v162, s[4:7], s60 offen \n"
    "buffer_load_dword v6, v162, s[4:7], s61 offen \n"
    "buffer_load_dword v7, v162, s[4:7], s62 offen \n"
    "buffer_load_dword v8, v162, s[4:7], s63 offen \n"
    "buffer_load_dword v9, v162, s[4:7], s64 offen \n"
    ".Lk4:\n"
    "s_waitcnt lgkmcnt(0)\n"
    "s_load_dword s53, s[16:17], 0\n"
    "s_branch .LkBrest\n"
    ".LkA:\n"
    "s_sub_u32 s53, s45, -104\n"
    "s_lshr_b32 s47, s53, 3\n"
    "s_and_b32 s51, s53, 7\n"
    "s_lshl_b32 s53, s51, 1\n"
    "s_add_u32 s53, s53, s47\n"
    "s_sub_u32 s53, s53, 11\n"
    "s_cmp_ge_u32 s47, 11\n"
    "s_cselect_b32 s52, s53, s47\n"
    "s_cselect_b32 s51, 8, s51\n"
    "s_cmp_ge_u32 s52, 11\n"
    "s_cbranch_scc0 .Lk42\n"
    "s_endpgm\n"
    ".Lk42:\n"
    "s_mul_i32 s53, s52, 6\n"
    "s_min_u32 s53, s53, 58\n"
    "s_lshl_b32 s46, s51, 6\n"
    "s_add_u32 s46, s46, s53\n"
    "s_and_b32 s49, s44, 3\n"
    "s_lshr_b32 s48, s44, 2\n"
    "s_lshr_b32 s53, s44, 1\n"
    "s_and_b32 s54, s44, 1\n"
    "s_cmp_ge_u32 s44, 8\n"
    "s_cselect_b32 s49, s53, s49\n"
    "s_cselect_b32 s48, s54, s48\n"
    "s_add_u32 s50, s46, s49\n"
    "s_lshl_b32 s53, s50, 1\n"
    "s_add_u32 s54, s53, s48\n"
    "s_lshl_b32 s54, s54, 7\n"
    "v_lshl_add_u32 v165, v1, 1, s54\n"
    "s_and_b32 s53, s53, -4\n"
    "s_lshl_b32 s55, s46, 1\n"
    "s_lshl_b32 s54, s51, 7\n"
    "s_add_u32 s54, s54, 128\n"
    "s_waitcnt lgkmcnt(0)\n"
    "s_load_dword s57, s[40:41], s53\n"
    "s_load_dword s58, s[42:43], s53\n"
    "s_load_dword s56, s[16:17], s54\n"
    "global_load_short_d16_hi v2, v165, s[28:29]\n"
    "global_load_short_d16_hi v3, v165, s[30:31]\n"
    "global_load_short_d16_hi v6, v165, s[32:33]\n"
    "global_load_short_d16_hi v8, v165, s[34:35]\n"
    "global_load_short_d16_hi v4, v165, s[36:37]\n"
    "global_load_short_d16_hi v5, v165, s[38:39]\n"
    "v_mov_b32 v26, 0\n"
    "v_mov_b32 v27, 0\n"
    "v_mov_b32 v28, 0\n"
    "v_mov_b32 v29, 0\n"
    "v_min_u32 v30, 587, v0\n"
    "v_lshlrev_b32 v30, 5, v30\n"
    "v_add_u32 v30, 98816, v30\n"
    "s_mul_i32 s59, s49, 4608\n"
    "s_add_u32 s59, s59, 117632\n"
    "v_and_b32 v162, 31, v0\n"
    "v_lshl_add_u32 v31, v162, 4, s59\n"
    "ds_write_b128 v30, v[26:29]\n"
    "ds_write_b128 v30, v[26:29] offset:16\n"
    "ds_write_b128 v31, v[26:29]\n"
    "v_bfe_u32 v150, v0, 3, 2\n"
    "v_and_b32 v148, 3, v0\n"
    "v_lshl_or_b32 v150, v150, 2, v148\n"
    "v_bfe_u32 v149, v0, 2, 1\n"
    "v_min_u32 v148, 7, v150\n"
    "v_cmp_eq_u32 vcc, 1, v149\n"
    "v_lshlrev_b32 v148, 2, v148\n"
    "s_mov_b32 s53, 3703290521\n"
    "v_bfe_u32 v148, s53, v148, 4\n"
    "v_cndmask_b32 v161, v150, v148, vcc\n"
    "v_and_b32 v148, 2, v0\n"
    "s_lshl_b32 s54, s44, 8\n"
    "v_add_u32 v148, v148, v149\n"
    "s_cmp_ge_u32 s44, 8\n"
    "v_and_b32 v149, 1, v0\n"
    "v_cmp_le_u32 vcc, 12, v150\n"
    "s_cselect_b32 s60, 10, 3\n"
    "v_lshl_add_u32 v148, v148, 1, v149\n"
    "s_and_b32 s60, s60, s44\n"
    "v_add_u32 v148, 14, v148\n"
    "s_lshl_b32 s60, s60, 8\n"
    "v_bfe_u32 v158, v0, 5, 1\n"
    "v_cndmask_b32 v161, v161, v148, vcc\n"
    "v_lshl_add_u32 v160, v167, 4, s60\n"
    "s_movk_i32 s53, 3088\n"
    "v_lshl_add_u32 v148, v158, 4, s54\n"
    "s_mul_i32 s60, s49, 3136\n"
    "s_mov_b32 m0, s54\n"
    "v_mad_u32_u24 v161, v161, s53, v148\n"
    "v_mad_u32_u24 v162, v162, s53, v148\n"
    "v_mad_u32_u24 v160, v166, s53, v160\n"
    "s_add_u32 s60, s60, 98816\n"
    "v_lshlrev_b32 v157, 2, v1\n"
    "s_cmp_eq_u32 s48, 1\n"
    "s_cbranch_scc0 .Lk43\n"
    "v_lshlrev_b32 v156, 4, v1\n"
    "s_add_u32 s53, s60, 1136\n"
    "v_sub_u32 v156, s53, v156\n"
    "v_bfe_u32 v165, v0, 1, 4\n"
    "v_lshl_add_u32 v159, v158, 4, v165\n"
    "v_lshl_add_u32 v155, v158, 5, v165\n"
    "v_mul_u32_u24 v165, 48, v149\n"
    "v_sub_u32 v159, v165, v159\n"
    "v_lshl_add_u32 v159, v159, 2, s60\n"
    "v_lshlrev_b32 v155, 1, v155\n"
    "v_sub_u32 v155, v149, v155\n"
    "v_lshl_add_u32 v155, v155, 1, s60\n"
    ".Lk43:\n"
    "s_mov_b32 s63, 84148480\n"
    "s_mov_b32 s64, 117834498\n"
    "s_cmp_lt_u32 s44, 4\n"
    "s_cbranch_scc1 .Lk46\n"
    "v_lshlrev_b32 v165, 2, v166\n"
    "v_lshrrev_b32 v153, 1, v167\n"
    "s_add_u32 s61, s60, 2368\n"
    "v_sub_u32 v153, v165, v153\n"
    "v_mul_u32_u24 v152, 305, v149\n"
    "v_sub_u32 v153, v153, v152\n"
    "s_add_u32 s62, s60, 1888\n"
    "v_lshl_add_u32 v153, v153, 2, s62\n"
    "v_lshl_add_u32 v154, v167, 5, s59\n"
    "v_sub_u32 v151, v165, v167\n"
    "s_mul_i32 s62, s50, 4224\n"
    "v_lshl_add_u32 v154, v166, 4, v154\n"
    "v_lshl_add_u32 v151, v151, 2, s61\n"
    "v_subrev_u32 v152, 640, v153\n"
    "s_add_u32 s62, s62, 3145728\n"
    "s_cmp_lt_u32 s44, 8\n"
    "s_cbranch_scc1 .Lk44\n"
    "v_and_b32 v165, 127, v0\n"
    "s_mul_i32 s53, s47, 94\n"
    "s_sub_i32 s54, 1024, s53\n"
    "s_min_i32 s54, s54, 94\n"
    "v_cmp_gt_i32 vcc, s54, v165\n"
    "v_mul_u32_u24 v165, 1152, v165\n"
    "s_and_b32 s54, s44, 2\n"
    "s_lshl_b32 s54, s54, 9\n"
    "s_add_u32 s53, s53, s54\n"
    "s_mul_i32 s53, s53, 1152\n"
    "v_add_u32 v165, s53, v165\n"
    "s_and_b32 s54, s45, 7\n"
    "s_lshl_b32 s54, s54, 7\n"
    "s_mov_b64 exec, vcc\n"
    "buffer_load_dword v13, v165, s[24:27], s54 offen\n"
    "s_cmp_lt_u32 s54, 768\n"
    "s_cselect_b64 exec, exec, 0\n"
    "buffer_load_dword v17, v165, s[24:27], 0 offen offset:1024\n"
    "s_mov_b64 exec, -1\n"
    "s_waitcnt lgkmcnt(0)\n"
    "s_barrier\n"
    "s_waitcnt vmcnt(2)\n"
    "s_branch .Lk45\n"
    ".Lk46:\n"
    "s_mul_i32 s54, s47, 21\n"
    "s_add_u32 s53, s44, 1\n"
    "v_add_u32 v163, s54, v0\n"
    "s_cmp_eq_u32 s44, 0\n"
    "s_cselect_b32 s54, 8, 12\n"
    "s_add_u32 s54, s54, s53\n"
    "v_lshl_add_u32 v150, v166, 3, s54\n"
    "s_lshl_b32 s53, s53, 1\n"
    "v_sub_u32 v150, v150, v167\n"
    "s_add_u32 s53, s53, 1\n"
    "v_med3_i32 v150, v150, 0, 31\n"
    "s_bfm_b32 s53, s53, 20\n"
    "v_lshrrev_b32 v150, v150, s53\n"
    "v_bfe_u32 v26, v150, 0, 2\n"
    "v_bfe_u32 v27, v150, 2, 2\n"
    "v_bfe_u32 v28, v150, 4, 2\n"
    "v_bfe_u32 v29, v150, 6, 2\n"
    "v_and_b32 v163, 255, v163\n"
    "v_mul_u32_u24 v26, 32769, v26\n"
    "v_mul_u32_u24 v27, 32769, v27\n"
    "v_mul_u32_u24 v28, 32769, v28\n"
    "v_mul_u32_u24 v29, 32769, v29\n"
    "v_mul_u32_u24 v164, 4608, v163\n"
    "v_and_b32 v26, 65537, v26\n"
    "v_and_b32 v27, 65537, v27\n"
    "v_and_b32 v28, 65537, v28\n"
    "v_and_b32 v29, 65537, v29\n"
    "v_lshlrev_b32 v165, 4, v0\n"
    "v_lshlrev_b32 v163, 3, v163\n"
    "v_mul_u32_u24 v26, 16256, v26\n"
    "v_mul_u32_u24 v27, 16256, v27\n"
    "v_mul_u32_u24 v28, 16256, v28\n"
    "v_mul_u32_u24 v29, 16256, v29\n"
    "v_add_u32 v165, 145280, v165\n"
    "v_add_u32 v163, 118144, v163\n"
    "ds_write_b128 v165, v[26:29]\n"
    ".Lk44:\n"
    "s_waitcnt lgkmcnt(0)\n"
    "s_barrier\n"
    "s_waitcnt vmcnt(0)\n"
    ".Lk45:\n"
    "s_nop 0\n"
    "s_and_b32 s53, s50, 1\n"
    "v_pk_mul_f32 v[120:121], v[4:5], v[6:7] op_sel_hi:[1,0]\n"
    "s_lshl_b32 s53, s53, 4\n"
    "v_pk_fma_f32 v[118:119], v[4:5], v[8:9], v[120:121] op_sel:[1,0,0] op_sel_hi:[0,0,1] neg_lo:[0,1,0]\n"
    "s_lshr_b32 s57, s57, s53\n"
    "v_max_f32 v120, abs(v118), abs(v119)\n"
    "s_lshl_b32 s57, s57, 16\n"
    "v_mov_b32 v105, s57\n"
    "s_mov_b32 s54, 0x3fb8aa3b\n"
    "v_mul_f32 v7, s54, v105\n"
    "v_max_u32_dpp v120, v120, v120 quad_perm:[1,0,3,2] row_mask:0xf bank_mask:0xf bound_ctrl:1\n"
    "v_fma_f32 v9, v105, s54, -v7\n"
    "v_exp_f32_e64 v7, v7\n"
    "v_fmamk_f32 v9, v105, 0x32a57060, v9\n"
    "v_max_u32_dpp v120, v120, v120 quad_perm:[2,3,0,1] row_mask:0xf bank_mask:0xf bound_ctrl:1\n"
    "v_mul_f32 v9, 0x3f317218, v9\n"
    "v_fma_f32 v7, v7, v9, v7\n"
    "v_cvt_pk_bf16_f32 v104, 0, v7\n"
    "v_max_u32_dpp v120, v120, v120 row_half_mirror row_mask:0xf bank_mask:0xf bound_ctrl:1\n"
    "v_pk_mul_f32 v[2:3], v[2:3], v[104:105] op_sel_hi:[1,0]\n"
    "v_mul_f32 v108, 0x3e22f983, v3\n"
    "v_mul_f32 v109, 0x40a2f983, v3\n"
    "v_mul_f32 v106, 0x3fb8aa3b, v2\n"
    "v_fract_f32 v108, v108\n"
    "v_mul_f32 v107, 0x4238aa3b, v2\n"
    "v_fract_f32 v109, v109\n"
    "v_max_u32_dpp v120, v120, v120 row_mirror row_mask:0xf bank_mask:0xf bound_ctrl:1\n"
    "v_exp_f32 v106, v106\n"
    "v_cos_f32 v110, v108\n"
    "v_sin_f32 v112, v108\n"
    "v_exp_f32 v107, v107\n"
    "v_cos_f32 v111, v109\n"
    "v_sin_f32_e64 v113, v109\n"
    "v_max_u32_dpp v120, v120, v120 row_bcast:15 row_mask:0xa bank_mask:0xf\n"
    "v_pk_mul_f32 v[110:111], v[110:111], v[106:107]\n"
    "v_pk_mul_f32 v[112:113], v[112:113], v[106:107]\n"
    "v_cvt_pk_f16_f32 v86, v110, v112\n"
    "v_cvt_pk_f16_f32 v91, v111, v113\n"
    "v_pk_mul_f32 v[114:115], v[112:113], v[112:113]\n"
    "v_pk_add_f32 v[116:117], v[110:111], v[110:111]\n"
    "s_lshr_b32 s58, s58, s53\n"
    "v_pk_fma_f32 v[110:111], v[110:111], v[110:111], v[114:115] neg_lo:[0,0,1] neg_hi:[0,0,1]\n"
    "v_pk_mul_f32 v[112:113], v[112:113], v[116:117]\n"
    "v_max_u32_dpp v120, v120, v120 row_bcast:31 row_mask:0xc bank_mask:0xf\n"
    "s_lshl_b32 s58, s58, 16\n"
    "s_mul_i32 s56, s56, 1664525\n"
    "v_cvt_pk_f16_f32 v87, v110, v112\n"
    "v_cvt_pk_f16_f32 v92, v111, v113\n"
    "v_pk_mul_f32 v[114:115], v[112:113], v[112:113]\n"
    "v_pk_add_f32 v[116:117], v[110:111], v[110:111]\n"
    "s_add_u32 s56, s56, 1013904223\n"
    "v_pk_fma_f32 v[110:111], v[110:111], v[110:111], v[114:115] neg_lo:[0,0,1] neg_hi:[0,0,1]\n"
    "v_pk_mul_f32 v[112:113], v[112:113], v[116:117]\n"
    "v_readlane_b32 s57, v120, 63\n"
    "v_cvt_pk_f16_f32 v88, v110, v112\n"
    "v_cvt_pk_f16_f32 v93, v111, v113\n"
    "v_pk_mul_f32 v[114:115], v[112:113], v[112:113]\n"
    "v_pk_add_f32 v[116:117], v[110:111], v[110:111]\n"
    "s_and_b32 s57, s57, 2139095040\n"
    "v_pk_fma_f32 v[110:111], v[110:111], v[110:111], v[114:115] neg_lo:[0,0,1] neg_hi:[0,0,1]\n"
    "v_pk_mul_f32 v[112:113], v[112:113], v[116:117]\n"
    "s_max_u32 s57, s57, 67108864\n"
    "v_cvt_pk_f16_f32 v89, v110, v112\n"
    "v_cvt_pk_f16_f32 v94, v111, v113\n"
    "v_pk_mul_f32 v[114:115], v[112:113], v[112:113]\n"
    "v_pk_add_f32 v[116:117], v[110:111], v[110:111]\n"
    "s_min_u32 s57, s57, 2097152000\n"
    "v_pk_fma_f32 v[110:111], v[110:111], v[110:111], v[114:115] neg_lo:[0,0,1] neg_hi:[0,0,1]\n"
    "v_pk_mul_f32 v[112:113], v[112:113], v[116:117]\n"
    "s_sub_u32 s57, s57, 33554432\n"
    "v_cvt_pk_f16_f32 v90, v110, v112\n"
    "v_cvt_pk_f16_f32 v95, v111, v113\n"
    "v_pk_mul_f32 v[114:115], v[112:113], v[112:113]\n"
    "v_pk_add_f32 v[116:117], v[110:111], v[110:111]\n"
    "s_sub_u32 s53, 2130706432, s57\n"
    "v_pk_fma_f32 v[110:111], v[110:111], v[110:111], v[114:115] neg_lo:[0,0,1] neg_hi:[0,0,1]\n"
    "v_pk_mul_f32 v[112:113], v[112:113], v[116:117]\n"
    "v_mul_f32 v118, s53, v118\n"
    "v_mul_f32 v119, s53, v119\n"
    "v_cvt_pk_f16_f32 v96, v111, v113\n"
    "v_mul_f32_e64 v102, s57, v104\n"
    "v_cvt_pk_f16_f32 v1, v118, -v119\n"
    "v_mov_b32 v70, 15360\n"
    "v_pk_mul_f16 v149, v89, v90 op_sel_hi:[1,0]\n"
    "v_pk_fma_f16 v73, v89, v90, v149 op_sel:[1,1,0] op_sel_hi:[0,1,1] neg_lo:[0,1,0]\n"
    "ds_write_addtid_b32 v70 offset:0\n"
    "ds_write_addtid_b32 v89 offset:3088\n"
    "ds_write_addtid_b32 v90 offset:6176\n"
    "ds_write_addtid_b32 v73 offset:9264\n"
    "v_pk_mul_f16 v149, v89, v91 op_sel_hi:[1,0]\n"
    "v_pk_fma_f16 v75, v89, v91, v149 op_sel:[1,1,0] op_sel_hi:[0,1,1] neg_lo:[0,1,0]\n"
    "v_pk_mul_f16 v149, v90, v91 op_sel_hi:[1,0]\n"
    "v_pk_fma_f16 v76, v90, v91, v149 op_sel:[1,1,0] op_sel_hi:[0,1,1] neg_lo:[0,1,0]\n"
    "v_pk_mul_f16 v149, v73, v91 op_sel_hi:[1,0]\n"
    "v_pk_fma_f16 v77, v73, v91, v149 op_sel:[1,1,0] op_sel_hi:[0,1,1] neg_lo:[0,1,0]\n"
    "ds_write_addtid_b32 v91 offset:12352\n"
    "ds_write_addtid_b32 v75 offset:15440\n"
    "ds_write_addtid_b32 v76 offset:18528\n"
    "ds_write_addtid_b32 v77 offset:21616\n"
    "v_pk_mul_f16 v149, v89, v92 op_sel_hi:[1,0]\n"
    "v_pk_fma_f16 v79, v89, v92, v149 op_sel:[1,1,0] op_sel_hi:[0,1,1] neg_lo:[0,1,0]\n"
    "v_pk_mul_f16 v149, v90, v92 op_sel_hi:[1,0]\n"
    "v_pk_fma_f16 v80, v90, v92, v149 op_sel:[1,1,0] op_sel_hi:[0,1,1] neg_lo:[0,1,0]\n"
    "v_pk_mul_f16 v149, v73, v92 op_sel_hi:[1,0]\n"
    "v_pk_fma_f16 v81, v73, v92, v149 op_sel:[1,1,0] op_sel_hi:[0,1,1] neg_lo:[0,1,0]\n"
    "ds_write_addtid_b32 v92 offset:24704\n"
    "ds_write_addtid_b32 v79 offset:27792\n"
    "ds_write_addtid_b32 v80 offset:30880\n"
    "ds_write_addtid_b32 v81 offset:33968\n"
    "v_pk_mul_f16 v149, v91, v92 op_sel_hi:[1,0]\n"
    "v_pk_fma_f16 v82, v91, v92, v149 op_sel:[1,1,0] op_sel_hi:[0,1,1] neg_lo:[0,1,0]\n"
    "v_pk_mul_f16 v149, v75, v92 op_sel_hi:[1,0]\n"
    "v_pk_fma_f16 v83, v75, v92, v149 op_sel:[1,1,0] op_sel_hi:[0,1,1] neg_lo:[0,1,0]\n"
    "v_pk_mul_f16 v149, v76, v92 op_sel_hi:[1,0]\n"
    "v_pk_fma_f16 v84, v76, v92, v149 op_sel:[1,1,0] op_sel_hi:[0,1,1] neg_lo:[0,1,0]\n"
    "v_pk_mul_f16 v149, v77, v92 op_sel_hi:[1,0]\n"
    "v_pk_fma_f16 v85, v77, v92, v149 op_sel:[1,1,0] op_sel_hi:[0,1,1] neg_lo:[0,1,0]\n"
    "ds_write_addtid_b32 v82 offset:37056\n"
    "ds_write_addtid_b32 v83 offset:40144\n"
    "ds_write_addtid_b32 v84 offset:43232\n"
    "ds_write_addtid_b32 v85 offset:46320\n"
    "s_add_u32 m0, m0, 49408\n"
    "v_pk_mul_f16 v149, v89, v93 op_sel_hi:[1,0]\n"
    "v_pk_fma_f16 v166, v89, v93, v149 op_sel:[1,1,0] op_sel_hi:[0,1,1] neg_lo:[0,1,0]\n"
    "v_pk_mul_f16 v149, v90, v93 op_sel_hi:[1,0]\n"
    "v_pk_fma_f16 v158, v90, v93, v149 op_sel:[1,1,0] op_sel_hi:[0,1,1] neg_lo:[0,1,0]\n"
    "v_pk_mul_f16 v149, v73, v93 op_sel_hi:[1,0]\n"
    "v_pk_fma_f16 v150, v73, v93, v149 op_sel:[1,1,0] op_sel_hi:[0,1,1] neg_lo:[0,1,0]\n"
    "ds_write_addtid_b32 v93 offset:0\n"
    "ds_write_addtid_b32 v166 offset:3088\n"
    "ds_write_addtid_b32 v158 offset:6176\n"
    "ds_write_addtid_b32 v150 offset:9264\n"
    "v_pk_mul_f16 v149, v91, v93 op_sel_hi:[1,0]\n"
    "v_pk_fma_f16 v167, v91, v93, v149 op_sel:[1,1,0] op_sel_hi:[0,1,1] neg_lo:[0,1,0]\n"
    "v_pk_mul_f16 v149, v75, v93 op_sel_hi:[1,0]\n"
    "v_pk_fma_f16 v166, v75, v93, v149 op_sel:[1,1,0] op_sel_hi:[0,1,1] neg_lo:[0,1,0]\n"
    "v_pk_mul_f16 v149, v76, v93 op_sel_hi:[1,0]\n"
    "v_pk_fma_f16 v158, v76, v93, v149 op_sel:[1,1,0] op_sel_hi:[0,1,1] neg_lo:[0,1,0]\n"
    "v_pk_mul_f16 v149, v77, v93 op_sel_hi:[1,0]\n"
    "v_pk_fma_f16 v150, v77, v93, v149 op_sel:[1,1,0] op_sel_hi:[0,1,1] neg_lo:[0,1,0]\n"
    "ds_write_addtid_b32 v167 offset:12352\n"
    "ds_write_addtid_b32 v166 offset:15440\n"
    "ds_write_addtid_b32 v158 offset:18528\n"
    "ds_write_addtid_b32 v150 offset:21616\n"
    "v_pk_mul_f16 v149, v92, v93 op_sel_hi:[1,0]\n"
    "v_pk_fma_f16 v167, v92, v93, v149 op_sel:[1,1,0] op_sel_hi:[0,1,1] neg_lo:[0,1,0]\n"
    "v_pk_mul_f16 v149, v79, v93 op_sel_hi:[1,0]\n"
    "v_pk_fma_f16 v166, v79, v93, v149 op_sel:[1,1,0] op_sel_hi:[0,1,1] neg_lo:[0,1,0]\n"
    "v_pk_mul_f16 v149, v80, v93 op_sel_hi:[1,0]\n"
    "v_pk_fma_f16 v158, v80, v93, v149 op_sel:[1,1,0] op_sel_hi:[0,1,1] neg_lo:[0,1,0]\n"
    "v_pk_mul_f16 v149, v81, v93 op_sel_hi:[1,0]\n"
    "v_pk_fma_f16 v150, v81, v93, v149 op_sel:[1,1,0] op_sel_hi:[0,1,1] neg_lo:[0,1,0]\n"
    "ds_write_addtid_b32 v167 offset:24704\n"
    "ds_write_addtid_b32 v166 offset:27792\n"
    "ds_write_addtid_b32 v158 offset:30880\n"
    "ds_write_addtid_b32 v150 offset:33968\n"
    "v_pk_mul_f16 v149, v82, v93 op_sel_hi:[1,0]\n"
    "v_pk_fma_f16 v167, v82, v93, v149 op_sel:[1,1,0] op_sel_hi:[0,1,1] neg_lo:[0,1,0]\n"
    "v_pk_mul_f16 v149, v83, v93 op_sel_hi:[1,0]\n"
    "v_pk_fma_f16 v166, v83, v93, v149 op_sel:[1,1,0] op_sel_hi:[0,1,1] neg_lo:[0,1,0]\n"
    "v_pk_mul_f16 v149, v84, v93 op_sel_hi:[1,0]\n"
    "v_pk_fma_f16 v158, v84, v93, v149 op_sel:[1,1,0] op_sel_hi:[0,1,1] neg_lo:[0,1,0]\n"
    "v_pk_mul_f16 v149, v85, v93 op_sel_hi:[1,0]\n"
    "v_pk_fma_f16 v150, v85, v93, v149 op_sel:[1,1,0] op_sel_hi:[0,1,1] neg_lo:[0,1,0]\n"
    "ds_write_addtid_b32 v167 offset:37056\n"
    "ds_write_addtid_b32 v166 offset:40144\n"
    "ds_write_addtid_b32 v158 offset:43232\n"
    "ds_write_addtid_b32 v150 offset:46320\n"
    "ds_read_b128 v[38:41], v162 offset:0\n"
    "ds_read_b128 v[42:45], v162 offset:32\n"
    "ds_read_b128 v[46:49], v162 offset:64\n"
    "ds_read_b128 v[50:53], v162 offset:96\n"
    "ds_read_b128 v[54:57], v162 offset:128\n"
    "ds_read_b128 v[58:61], v162 offset:160\n"
    "ds_read_b128 v[62:65], v162 offset:192\n"
    "ds_read_b128 v[66:69], v162 offset:224\n"
    "s_sub_u32 m0, m0, 49408\n"
    "v_pk_mul_f16 v149, v1, v86 op_sel_hi:[1,0]\n"
    "v_pk_fma_f16 v70, v1, v86, v149 op_sel:[1,1,0] op_sel_hi:[0,1,1] neg_hi:[0,1,0]\n"
    "v_pk_mul_f16 v149, v1, v87 op_sel_hi:[1,0]\n"
    "v_pk_fma_f16 v71, v1, v87, v149 op_sel:[1,1,0] op_sel_hi:[0,1,1] neg_hi:[0,1,0]\n"
    "v_pk_mul_f16 v149, v70, v87 op_sel_hi:[1,0]\n"
    "v_pk_fma_f16 v72, v70, v87, v149 op_sel:[1,1,0] op_sel_hi:[0,1,1] neg_hi:[0,1,0]\n"
    "ds_write_addtid_b32 v1 offset:0\n"
    "ds_write_addtid_b32 v70 offset:3088\n"
    "ds_write_addtid_b32 v71 offset:6176\n"
    "ds_write_addtid_b32 v72 offset:9264\n"
    "v_pk_mul_f16 v149, v1, v88 op_sel_hi:[1,0]\n"
    "v_pk_fma_f16 v73, v1, v88, v149 op_sel:[1,1,0] op_sel_hi:[0,1,1] neg_hi:[0,1,0]\n"
    "v_pk_mul_f16 v149, v70, v88 op_sel_hi:[1,0]\n"
    "v_pk_fma_f16 v166, v70, v88, v149 op_sel:[1,1,0] op_sel_hi:[0,1,1] neg_hi:[0,1,0]\n"
    "v_pk_mul_f16 v149, v71, v88 op_sel_hi:[1,0]\n"
    "v_pk_fma_f16 v74, v71, v88, v149 op_sel:[1,1,0] op_sel_hi:[0,1,1] neg_hi:[0,1,0]\n"
    "v_pk_mul_f16 v149, v72, v88 op_sel_hi:[1,0]\n"
    "v_pk_fma_f16 v150, v72, v88, v149 op_sel:[1,1,0] op_sel_hi:[0,1,1] neg_hi:[0,1,0]\n"
    "ds_write_addtid_b32 v73 offset:12352\n"
    "ds_write_addtid_b32 v166 offset:15440\n"
    "ds_write_addtid_b32 v74 offset:18528\n"
    "ds_write_addtid_b32 v150 offset:21616\n"
    "v_pk_mul_f16 v149, v1, v89 op_sel_hi:[1,0]\n"
    "v_pk_fma_f16 v75, v1, v89, v149 op_sel:[1,1,0] op_sel_hi:[0,1,1] neg_hi:[0,1,0]\n"
    "v_pk_mul_f16 v149, v1, v94 op_sel_hi:[1,0]\n"
    "v_pk_fma_f16 v166, v1, v94, v149 op_sel:[1,1,0] op_sel_hi:[0,1,1] neg_hi:[0,1,0]\n"
    "v_pk_mul_f16 v149, v71, v94 op_sel_hi:[1,0]\n"
    "v_pk_fma_f16 v158, v71, v94, v149 op_sel:[1,1,0] op_sel_hi:[0,1,1] neg_hi:[0,1,0]\n"
    "v_pk_mul_f16 v149, v73, v94 op_sel_hi:[1,0]\n"
    "v_pk_fma_f16 v76, v73, v94, v149 op_sel:[1,1,0] op_sel_hi:[0,1,1] neg_hi:[0,1,0]\n"
    "ds_write_addtid_b32 v75 offset:24704\n"
    "ds_write_addtid_b32 v166 offset:27792\n"
    "ds_write_addtid_b32 v158 offset:30880\n"
    "ds_write_addtid_b32 v76 offset:33968\n"
    "v_pk_mul_f16 v149, v74, v94 op_sel_hi:[1,0]\n"
    "v_pk_fma_f16 v167, v74, v94, v149 op_sel:[1,1,0] op_sel_hi:[0,1,1] neg_hi:[0,1,0]\n"
    "v_pk_mul_f16 v149, v75, v94 op_sel_hi:[1,0]\n"
    "v_pk_fma_f16 v77, v75, v94, v149 op_sel:[1,1,0] op_sel_hi:[0,1,1] neg_hi:[0,1,0]\n"
    "v_pk_mul_f16 v149, v73, v95 op_sel_hi:[1,0]\n"
    "v_pk_fma_f16 v158, v73, v95, v149 op_sel:[1,1,0] op_sel_hi:[0,1,1] neg_hi:[0,1,0]\n"
    "v_pk_mul_f16 v149, v75, v95 op_sel_hi:[1,0]\n"
    "v_pk_fma_f16 v78, v75, v95, v149 op_sel:[1,1,0] op_sel_hi:[0,1,1] neg_hi:[0,1,0]\n"
    "ds_write_addtid_b32 v167 offset:37056\n"
    "ds_write_addtid_b32 v77 offset:40144\n"
    "ds_write_addtid_b32 v158 offset:43232\n"
    "ds_write_addtid_b32 v78 offset:46320\n"
    "v_pk_mul_f16 v149, v76, v95 op_sel_hi:[1,0]\n"
    "v_pk_fma_f16 v167, v76, v95, v149 op_sel:[1,1,0] op_sel_hi:[0,1,1] neg_hi:[0,1,0]\n"
    "v_pk_mul_f16 v149, v77, v95 op_sel_hi:[1,0]\n"
    "v_pk_fma_f16 v79, v77, v95, v149 op_sel:[1,1,0] op_sel_hi:[0,1,1] neg_hi:[0,1,0]\n"
    "v_pk_mul_f16 v149, v75, v96 op_sel_hi:[1,0]\n"
    "v_pk_fma_f16 v158, v75, v96, v149 op_sel:[1,1,0] op_sel_hi:[0,1,1] neg_hi:[0,1,0]\n"
    "v_pk_mul_f16 v149, v77, v96 op_sel_hi:[1,0]\n"
    "v_pk_fma_f16 v150, v77, v96, v149 op_sel:[1,1,0] op_sel_hi:[0,1,1] neg_hi:[0,1,0]\n"
    "ds_write_addtid_b32 v167 offset:49408\n"
    "ds_write_addtid_b32 v79 offset:52496\n"
    "ds_write_addtid_b32 v158 offset:55584\n"
    "ds_write_addtid_b32 v150 offset:58672\n"
    "v_pk_mul_f16 v149, v78, v96 op_sel_hi:[1,0]\n"
    "v_pk_fma_f16 v167, v78, v96, v149 op_sel:[1,1,0] op_sel_hi:[0,1,1] neg_hi:[0,1,0]\n"
    "v_pk_mul_f16 v149, v79, v96 op_sel_hi:[1,0]\n"
    "v_pk_fma_f16 v166, v79, v96, v149 op_sel:[1,1,0] op_sel_hi:[0,1,1] neg_hi:[0,1,0]\n"
    "ds_write_addtid_b32 v167 offset:61760\n"
    "ds_write_addtid_b32 v166 offset:64848\n"
    "ds_read_b128 v[70:73], v161 offset:0\n"
    "ds_read_b128 v[74:77], v161 offset:32\n"
    "ds_read_b128 v[78:81], v161 offset:64\n"
    "ds_read_b128 v[82:85], v161 offset:96\n"
    "ds_read_b128 v[86:89], v161 offset:128\n"
    "ds_read_b128 v[90:93], v161 offset:160\n"
    "ds_read_b128 v[94:97], v161 offset:192\n"
    "ds_read_b128 v[98:101], v161 offset:224\n"
    "s_cmp_ge_u32 s44, 4\n"
    "s_cbranch_scc1 .Lk47\n"
    "buffer_load_dwordx3 v[10:12], v164, s[24:27], s55 offen offset:0 \n"
    "buffer_load_dwordx3 v[14:16], v164, s[24:27], s55 offen offset:1152 \n"
    "buffer_load_dwordx3 v[18:20], v164, s[24:27], s55 offen offset:2304 \n"
    "buffer_load_dwordx3 v[22:24], v164, s[24:27], s55 offen offset:3456 \n"
    ".Lk47:\n"
    "v_mov_b32 v165, s60\n"
    "v_mov_b32 v1, 100016\n"
    "s_cmp_eq_u32 s48, 1\n"
    "s_cbranch_scc1 .Lk48\n"
    "s_setprio 1\n"
    "s_waitcnt lgkmcnt(7)\n"
    "v_mfma_f32_32x32x16_f16 v[104:119], v[70:73], v[38:41], 0\n"
    "s_waitcnt lgkmcnt(6)\n"
    "v_mfma_f32_32x32x16_f16 v[104:119], v[74:77], v[42:45], v[104:119]\n"
    "s_waitcnt lgkmcnt(5)\n"
    "v_mfma_f32_32x32x16_f16 v[104:119], v[78:81], v[46:49], v[104:119]\n"
    "s_waitcnt lgkmcnt(4)\n"
    "v_mfma_f32_32x32x16_f16 v[104:119], v[82:85], v[50:53], v[104:119]\n"
    "s_waitcnt lgkmcnt(3)\n"
    "v_mfma_f32_32x32x16_f16 v[104:119], v[86:89], v[54:57], v[104:119]\n"
    "s_waitcnt lgkmcnt(2)\n"
    "v_mfma_f32_32x32x16_f16 v[104:119], v[90:93], v[58:61], v[104:119]\n"
    "s_waitcnt lgkmcnt(1)\n"
    "v_mfma_f32_32x32x16_f16 v[104:119], v[94:97], v[62:65], v[104:119]\n"
    "s_waitcnt lgkmcnt(0)\n"
    "v_mfma_f32_32x32x16_f16 v[104:119], v[98:101], v[66:69], v[104:119]\n"
    "s_setprio 0\n"
    "s_nop 4\n"
    "v_mul_f32 v104, v102, v104\n"
    "v_mul_f32 v105, v102, v105\n"
    "v_mul_f32 v106, v102, v106\n"
    "v_mul_f32 v107, v102, v107\n"
    "v_mul_f32 v108, v102, v108\n"
    "v_mul_f32 v109, v102, v109\n"
    "v_mul_f32 v110, v102, v110\n"
    "v_mul_f32 v111, v102, v111\n"
    "v_mul_f32 v112, v102, v112\n"
    "v_mul_f32 v116, v102, v116\n"
    "v_mul_f32 v117, v102, v117\n"
    "v_mul_f32 v118, v102, v118\n"
    "v_mul_f32 v119, v102, v119\n"
    "s_mov_b64 exec, 1\n"
    "v_add_f32 v104, s58, v104\n"
    "s_mov_b64 exec, -1\n"
    "ds_write_b128 v160, v[104:107] offset:0\n"
    "ds_write_b128 v160, v[108:111] offset:12352\n"
    "ds_write_b128 v160, v[116:119] offset:37056\n"
    "ds_write_b32 v160, v112 offset:24704\n"
    "ds_write_b32 v165, v1 offset:3132\n"
    "s_setprio 0\n"
    "s_cmp_ge_u32 s44, 8\n"
    "s_cbranch_scc1 .Lk51\n"
    "s_add_u32 s53, s55, 1179648\n"
    "buffer_load_dwordx3 v[70:72], v164, s[24:27], s53 offen offset:0 \n"
    "buffer_load_dwordx3 v[74:76], v164, s[24:27], s53 offen offset:1152 \n"
    "buffer_load_dwordx3 v[78:80], v164, s[24:27], s53 offen offset:2304 \n"
    "buffer_load_dwordx3 v[82:84], v164, s[24:27], s53 offen offset:3456 \n"
    "s_waitcnt vmcnt(4)\n"
    "s_nop 0\n"
    "s_nop 0\n"
    "v_perm_b32 v26, v14, v10, s63\n"
    "v_perm_b32 v27, v22, v18, s63\n"
    "v_perm_b32 v28, v14, v10, s64\n"
    "v_perm_b32 v29, v22, v18, s64\n"
    "v_perm_b32 v30, v15, v11, s63\n"
    "v_perm_b32 v31, v23, v19, s63\n"
    "v_perm_b32 v32, v15, v11, s64\n"
    "v_perm_b32 v33, v23, v19, s64\n"
    "v_perm_b32 v34, v16, v12, s63\n"
    "v_perm_b32 v35, v24, v20, s63\n"
    "v_perm_b32 v36, v16, v12, s64\n"
    "v_perm_b32 v37, v24, v20, s64\n"
    "v_mov_b32 v10, 100016\n"
    "v_mov_b32_e64 v11, 1\n"
    "ds_write_b64 v163, v[26:27] offset:0\n"
    "ds_write_b64 v163, v[28:29] offset:4608\n"
    "ds_write_b64 v163, v[30:31] offset:9216\n"
    "ds_write_b64 v163, v[32:33] offset:13824\n"
    "ds_write_b64 v163, v[34:35] offset:18432\n"
    "ds_write_b64 v163, v[36:37] offset:23040\n"
    "s_mov_b64 exec, 1\n"
    "ds_add_u32 v10, v11\n"
    "s_mov_b64 exec, -1\n"
    "s_waitcnt vmcnt(2)\n"
    "v_perm_b32 v26, v74, v70, s63\n"
    "s_waitcnt vmcnt(0)\n"
    "v_perm_b32 v27, v82, v78, s63\n"
    "v_perm_b32 v28, v74, v70, s64\n"
    "v_perm_b32 v29, v82, v78, s64\n"
    "v_perm_b32 v30, v75, v71, s63\n"
    "v_perm_b32 v31, v83, v79, s63\n"
    "v_perm_b32 v32, v75, v71, s64\n"
    "v_perm_b32 v33, v83, v79, s64\n"
    "v_perm_b32 v34, v76, v72, s63\n"
    "v_perm_b32 v35, v84, v80, s63\n"
    "v_perm_b32 v36, v76, v72, s64\n"
    "v_perm_b32 v37, v84, v80, s64\n"
    "v_mov_b32 v70, 100020\n"
    "v_mov_b32_e64 v71, 1\n"
    "ds_write_b64 v163, v[26:27] offset:2048\n"
    "ds_write_b64 v163, v[28:29] offset:6656\n"
    "ds_write_b64 v163, v[30:31] offset:11264\n"
    "ds_write_b64 v163, v[32:33] offset:15872\n"
    "ds_write_b64 v163, v[34:35] offset:20480\n"
    "ds_write_b64 v163, v[36:37] offset:25088\n"
    "s_mov_b64 exec, 1\n"
    "ds_add_u32 v70, v71\n"
    "s_mov_b64 exec, -1\n"
    "s_branch .Lk50\n"
    ".Lk51:\n"
    "s_mov_b32 s66, 32768\n"
    ".Lk52:\n"
    "ds_read_b32 v131, v165 offset:1196\n"
    "s_waitcnt lgkmcnt(0)\n"
    "v_cmp_gt_u32 vcc, 1, v131\n"
    "s_cbranch_vccz .Lk53\n"
    "s_sleep 1\n"
    "s_sub_u32 s66, s66, 1\n"
    "s_cbranch_scc0 .Lk52\n"
    ".Lk53:\n"
    "s_branch .Lk49\n"
    ".Lk48:\n"
    "s_mov_b32 s66, 32768\n"
    ".Lk54:\n"
    "ds_read_b32 v131, v165 offset:3132\n"
    "s_waitcnt lgkmcnt(0)\n"
    "v_cmp_gt_u32 vcc, 1, v131\n"
    "s_cbranch_vccz .Lk55\n"
    "s_sleep 1\n"
    "s_sub_u32 s66, s66, 1\n"
    "s_cbranch_scc0 .Lk54\n"
    ".Lk55:\n"
    "ds_read_b128 v[120:123], v160 offset:0\n"
    "ds_read_b128 v[124:127], v160 offset:12352\n"
    "ds_read_b128 v[132:135], v160 offset:37056\n"
    "ds_read_b32 v128, v160 offset:24704\n"
    "s_setprio 1\n"
    "v_mfma_f32_32x32x16_f16 v[104:119], v[70:73], v[38:41], 0\n"
    "v_mfma_f32_32x32x16_f16 v[104:119], v[74:77], v[42:45], v[104:119]\n"
    "v_mfma_f32_32x32x16_f16 v[104:119], v[78:81], v[46:49], v[104:119]\n"
    "v_mfma_f32_32x32x16_f16 v[104:119], v[82:85], v[50:53], v[104:119]\n"
    "v_mfma_f32_32x32x16_f16 v[104:119], v[86:89], v[54:57], v[104:119]\n"
    "v_mfma_f32_32x32x16_f16 v[104:119], v[90:93], v[58:61], v[104:119]\n"
    "v_mfma_f32_32x32x16_f16 v[104:119], v[94:97], v[62:65], v[104:119]\n"
    "v_mfma_f32_32x32x16_f16 v[104:119], v[98:101], v[66:69], v[104:119]\n"
    "s_setprio 0\n"
    "s_waitcnt lgkmcnt(3)\n"
    "s_nop 3\n"
    "v_fmac_f32 v120, v102, v104\n"
    "v_fmac_f32 v121, v102, v105\n"
    "v_fmac_f32 v122, v102, v106\n"
    "v_fmac_f32 v123, v102, v107\n"
    "s_waitcnt lgkmcnt(2)\n"
    "v_fmac_f32 v124, v102, v108\n"
    "v_fmac_f32 v125, v102, v109\n"
    "v_fmac_f32 v126, v102, v110\n"
    "v_fmac_f32_e64 v127, v102, v111\n"
    "v_cvt_pk_bf16_f32 v41, v121, v120\n"
    "v_cvt_pk_bf16_f32 v40, v123, v122\n"
    "v_cvt_pk_bf16_f32 v39, v125, v124\n"
    "v_cvt_pk_bf16_f32 v38, v127, v126\n"
    "s_waitcnt lgkmcnt(1)\n"
    "v_fmac_f32 v132, v102, v116\n"
    "v_fmac_f32 v133, v102, v117\n"
    "v_fmac_f32 v134, v102, v118\n"
    "v_fmac_f32 v135, v102, v119\n"
    "s_waitcnt lgkmcnt(0)\n"
    "v_fmac_f32_e64 v128, v102, v112\n"
    "v_cvt_pk_bf16_f32 v45, v122, v121\n"
    "v_cvt_pk_bf16_f32 v44, v124, v123\n"
    "v_cvt_pk_bf16_f32 v43, v126, v125\n"
    "v_cvt_pk_bf16_f32 v42, v128, v127\n"
    "v_cvt_pk_bf16_f32 v46, v132, v133\n"
    "v_cvt_pk_bf16_f32 v47, v134, v135\n"
    "ds_read_b32 v167, v1\n"
    "ds_write_b128 v156, v[38:41]\n"
    "ds_write_b128 v156, v[42:45] offset:1216\n"
    "ds_write_b32 v159, v46 offset:2560\n"
    "ds_write_b16 v155, v47 offset:3068\n"
    "ds_write_b16_d16_hi v155, v47 offset:3004\n"
    "s_mov_b64 exec, 1\n"
    "ds_write_b16_d16_hi v165, v41 offset:2368\n"
    "s_bfm_b64 exec, 1, 63\n"
    "ds_write_b32 v165, v42 offset:124\n"
    "ds_write_b16 v165, v42 offset:122\n"
    "ds_write_b32 v165, v42 offset:1340\n"
    "ds_write_b16_d16_hi v165, v46 offset:56\n"
    "ds_write_b16_d16_hi v165, v46 offset:54\n"
    "ds_write_b16_d16_hi v165, v46 offset:1272\n"
    "ds_write_b16_d16_hi v165, v46 offset:1274\n"
    "ds_write_b32 v165, v1 offset:1196\n"
    "s_mov_b64 exec, -1\n"
    "s_setprio 0\n"
    ".Lk49:\n"
    "s_nop 0\n"
    "s_cmp_ge_u32 s44, 8\n"
    "s_cbranch_scc1 .Lk57\n"
    "s_cmp_eq_u32 s52, 10\n"
    "s_cselect_b32 s53, s44, 6\n"
    "s_cmp_lt_u32 s53, 6\n"
    "s_cbranch_scc1 .Lk56\n"
    "v_lshlrev_b32 v165, 2, v157\n"
    "v_add_u32 v165, 145280, v165\n"
    "ds_read2_b32 v[62:63], v153 offset0:112 offset1:113\n"
    "ds_read2_b32 v[64:65], v153 offset0:114 offset1:115\n"
    "ds_read2_b32 v[66:67], v152 offset0:144 offset1:145\n"
    "ds_read2_b32 v[68:69], v152 offset0:146 offset1:147\n"
    "ds_read2_b32 v[70:71], v153 offset0:96 offset1:97\n"
    "ds_read2_b32 v[72:73], v153 offset0:98 offset1:99\n"
    "ds_read2_b32 v[58:59], v152 offset0:128 offset1:129\n"
    "ds_read2_b32 v[60:61], v152 offset0:130 offset1:131\n"
    "ds_read_b128 v[138:141], v165 offset:2048\n"
    "ds_read_b128 v[142:145], v165 offset:3072\n"
    "ds_read2_b32 v[110:111], v151 offset0:175 offset1:176\n"
    "ds_read2_b32 v[112:113], v151 offset0:177 offset1:178\n"
    "ds_read2_b32 v[114:115], v151 offset0:159 offset1:160\n"
    "ds_read2_b32 v[116:117], v151 offset0:161 offset1:162\n"
    "ds_read2_b32 v[118:119], v151 offset0:143 offset1:144\n"
    "ds_read2_b32 v[120:121], v151 offset0:145 offset1:146\n"
    "ds_read2_b32 v[122:123], v151 offset0:127 offset1:128\n"
    "ds_read2_b32 v[124:125], v151 offset0:129 offset1:130\n"
    "ds_read2_b32 v[126:127], v152 offset0:0 offset1:1\n"
    "ds_read2_b32 v[128:129], v152 offset0:2 offset1:3\n"
    "s_waitcnt lgkmcnt(14)\n"
    "v_cmp_gt_u32 vcc, 4, v167\n"
    "s_cbranch_vccz .Lk59\n"
    "s_mov_b32 s46, 32768\n"
    ".Lk60:\n"
    "ds_read_b32 v167, v1 offset:0\n"
    "s_waitcnt lgkmcnt(0)\n"
    "v_cmp_gt_u32_e64 vcc, 4, v167\n"
    "s_cbranch_vccz .Lk61\n"
    "s_sleep 1\n"
    "s_sub_u32 s46, s46, 1\n"
    "s_cbranch_scc0 .Lk60\n"
    ".Lk61:\n"
    ".Lk59:\n"
    "ds_read_b128 v[38:41], v154 offset:480\n"
    "ds_read_b128 v[78:81], v154 offset:480\n"
    "ds_read_b128 v[82:85], v154 offset:464\n"
    "ds_read_b128 v[74:77], v154 offset:992\n"
    "ds_read_b128 v[42:45], v154 offset:992\n"
    "ds_read_b128 v[46:49], v154 offset:1504\n"
    "ds_read_b128 v[50:53], v154 offset:2016\n"
    "ds_read_b128 v[54:57], v154 offset:416\n"
    "ds_read_b128 v[34:37], v154 offset:928\n"
    "ds_read2_b32 v[130:131], v151 offset0:80 offset1:81\n"
    "ds_read2_b32 v[132:133], v151 offset0:82 offset1:83\n"
    "ds_read2_b32 v[134:135], v151 offset0:64 offset1:65\n"
    "s_waitcnt lgkmcnt(11)\n"
    "v_mfma_f32_16x16x32_bf16 v[2:5], v[62:65], v[38:41], 0\n"
    "ds_read2_b32 v[136:137], v151 offset0:66 offset1:67\n"
    "v_mfma_f32_16x16x32_bf16 v[6:9], v[66:69], v[38:41], 0\n"
    "ds_read_b128 v[38:41], v154 offset:1440\n"
    "s_waitcnt lgkmcnt(5)\n"
    "v_mfma_f32_16x16x32_bf16 v[90:93], v[78:81], v[138:141], 0\n"
    "v_mfma_f32_16x16x32_bf16 v[94:97], v[82:85], v[142:145], 0\n"
    "ds_read_b128 v[82:85], v154 offset:1504\n"
    "v_mfma_f32_16x16x32_bf16 v[18:21], v[126:129], v[78:81], 0\n"
    "ds_read_b128 v[78:81], v154 offset:976\n"
    "v_mfma_f32_16x16x32_bf16 v[98:101], v[74:77], v[138:141], 0\n"
    "v_cvt_pk_bf16_f32 v94, v90, v94\n"
    "v_cvt_pk_bf16_f32 v95, v91, v95\n"
    "v_mfma_f32_16x16x32_bf16 v[6:9], v[62:65], v[42:45], v[6:9]\n"
    "v_cvt_pk_bf16_f32 v96, v92, v96\n"
    "v_cvt_pk_bf16_f32 v97, v93, v97\n"
    "v_mfma_f32_16x16x32_bf16 v[10:13], v[66:69], v[42:45], 0\n"
    "ds_read_b128 v[42:45], v154 offset:1952\n"
    "v_mfma_f32_16x16x32_bf16 v[18:21], v[94:97], v[110:113], v[18:21]\n"
    "v_mfma_f32_16x16x32_bf16 v[22:25], v[94:97], v[114:117], 0\n"
    "v_mfma_f32_16x16x32_bf16 v[26:29], v[94:97], v[118:121], 0\n"
    "v_mfma_f32_16x16x32_bf16 v[30:33], v[94:97], v[122:125], 0\n"
    "ds_read2_b32 v[122:123], v152 offset0:16 offset1:17\n"
    "ds_read2_b32 v[124:125], v152 offset0:18 offset1:19\n"
    "v_mfma_f32_16x16x32_bf16 v[22:25], v[126:129], v[74:77], v[22:25]\n"
    "ds_read_b128 v[74:77], v154 offset:1488\n"
    "s_waitcnt lgkmcnt(5)\n"
    "v_mfma_f32_16x16x32_bf16 v[106:109], v[82:85], v[138:141], 0\n"
    "s_waitcnt lgkmcnt(4)\n"
    "v_mfma_f32_16x16x32_bf16 v[102:105], v[78:81], v[142:145], 0\n"
    "ds_read_b128 v[78:81], v154 offset:2016\n"
    "v_mfma_f32_16x16x32_bf16 v[26:29], v[126:129], v[82:85], v[26:29]\n"
    "ds_read_b128 v[82:85], v154 offset:2000\n"
    "v_mfma_f32_16x16x32_bf16 v[10:13], v[62:65], v[46:49], v[10:13]\n"
    "v_cvt_pk_bf16_f32 v102, v98, v102\n"
    "v_cvt_pk_bf16_f32 v103, v99, v103\n"
    "v_mfma_f32_16x16x32_bf16 v[14:17], v[66:69], v[46:49], 0\n"
    "v_cvt_pk_bf16_f32 v104, v100, v104\n"
    "v_cvt_pk_bf16_f32 v105, v101, v105\n"
    "v_mfma_f32_16x16x32_bf16 v[14:17], v[62:65], v[50:53], v[14:17]\n"
    "v_mfma_f32_16x16x32_bf16 v[18:21], v[66:69], v[50:53], v[18:21]\n"
    "v_mfma_f32_16x16x32_bf16 v[22:25], v[102:105], v[110:113], v[22:25]\n"
    "v_mfma_f32_16x16x32_bf16 v[26:29], v[102:105], v[114:117], v[26:29]\n"
    "v_mfma_f32_16x16x32_bf16 v[30:33], v[102:105], v[118:121], v[30:33]\n"
    "ds_read2_b32 v[118:119], v151 offset0:16 offset1:17\n"
    "s_waitcnt lgkmcnt(3)\n"
    "v_mfma_f32_16x16x32_bf16 v[86:89], v[74:77], v[142:145], 0\n"
    "ds_read_b128 v[74:77], v154 offset:480\n"
    "s_waitcnt lgkmcnt(3)\n"
    "v_mfma_f32_16x16x32_bf16 v[90:93], v[78:81], v[138:141], 0\n"
    "ds_read_b128 v[138:141], v165 offset:0\n"
    "s_waitcnt lgkmcnt(3)\n"
    "v_mfma_f32_16x16x32_bf16 v[94:97], v[82:85], v[142:145], 0\n"
    "ds_read_b128 v[142:145], v165 offset:1024\n"
    "ds_read2_b32 v[120:121], v151 offset0:18 offset1:19\n"
    "v_mfma_f32_16x16x32_bf16 v[30:33], v[126:129], v[78:81], v[30:33]\n"
    "v_cvt_pk_bf16_f32 v86, v106, v86\n"
    "v_cvt_pk_bf16_f32 v87, v107, v87\n"
    "v_mfma_f32_16x16x32_bf16 v[2:5], v[70:73], v[54:57], v[2:5]\n"
    "v_cvt_pk_bf16_f32 v88, v108, v88\n"
    "v_cvt_pk_bf16_f32 v89, v109, v89\n"
    "v_mfma_f32_16x16x32_bf16 v[6:9], v[58:61], v[54:57], v[6:9]\n"
    "ds_read_b128 v[78:81], v154 offset:992\n"
    "v_cvt_pk_bf16_f32 v94, v90, v94\n"
    "v_mfma_f32_16x16x32_bf16 v[26:29], v[86:89], v[110:113], v[26:29]\n"
    "v_cvt_pk_bf16_f32 v95, v91, v95\n"
    "v_cvt_pk_bf16_f32 v96, v92, v96\n"
    "v_mfma_f32_16x16x32_bf16 v[30:33], v[86:89], v[114:117], v[30:33]\n"
    "ds_read2_b32 v[114:115], v151 offset0:32 offset1:33\n"
    "ds_read2_b32 v[116:117], v151 offset0:34 offset1:35\n"
    "v_mfma_f32_16x16x32_bf16 v[6:9], v[70:73], v[34:37], v[6:9]\n"
    "v_cvt_pk_bf16_f32 v97, v93, v97\n"
    "s_waitcnt lgkmcnt(6)\n"
    "v_mfma_f32_16x16x32_bf16 v[10:13], v[122:125], v[74:77], v[10:13]\n"
    "ds_read_b128 v[82:85], v154 offset:1504\n"
    "ds_read2_b32 v[126:127], v151 offset0:96 offset1:97\n"
    "v_mfma_f32_16x16x32_bf16 v[30:33], v[94:97], v[110:113], v[30:33]\n"
    "ds_read2_b32 v[110:111], v151 offset0:48 offset1:49\n"
    "s_waitcnt lgkmcnt(7)\n"
    "v_mfma_f32_16x16x32_bf16 v[98:101], v[74:77], v[138:141], 0\n"
    "ds_read2_b32 v[112:113], v151 offset0:50 offset1:51\n"
    "ds_read2_b32 v[128:129], v151 offset0:98 offset1:99\n"
    "v_mfma_f32_16x16x32_bf16 v[102:105], v[74:77], v[142:145], 0\n"
    "ds_read_b128 v[74:77], v154 offset:2016\n"
    "v_mfma_f32_16x16x32_bf16 v[10:13], v[58:61], v[34:37], v[10:13]\n"
    "s_waitcnt lgkmcnt(8)\n"
    "v_mfma_f32_16x16x32_bf16 v[106:109], v[78:81], v[138:141], 0\n"
    "v_cvt_pk_bf16_f32 v102, v98, v102\n"
    "v_cvt_pk_bf16_f32 v103, v99, v103\n"
    "v_mfma_f32_16x16x32_bf16 v[86:89], v[78:81], v[142:145], 0\n"
    "v_cvt_pk_bf16_f32 v104, v100, v104\n"
    "v_cvt_pk_bf16_f32 v105, v101, v105\n"
    "v_mfma_f32_16x16x32_bf16 v[14:17], v[122:125], v[78:81], v[14:17]\n"
    "ds_read_b128 v[78:81], v154 offset:464\n"
    "s_waitcnt lgkmcnt(7)\n"
    "v_mfma_f32_16x16x32_bf16 v[14:17], v[102:105], v[114:117], v[14:17]\n"
    "v_cvt_pk_bf16_f32 v86, v106, v86\n"
    "v_cvt_pk_bf16_f32 v87, v107, v87\n"
    "v_mfma_f32_16x16x32_bf16 v[18:21], v[102:105], v[118:121], v[18:21]\n"
    "v_cvt_pk_bf16_f32 v88, v108, v88\n"
    "s_waitcnt lgkmcnt(5)\n"
    "v_mfma_f32_16x16x32_bf16 v[90:93], v[82:85], v[138:141], 0\n"
    "v_cvt_pk_bf16_f32 v89, v109, v89\n"
    "s_waitcnt lgkmcnt(3)\n"
    "v_mfma_f32_16x16x32_bf16 v[10:13], v[102:105], v[110:113], v[10:13]\n"
    "v_mfma_f32_16x16x32_bf16 v[14:17], v[86:89], v[110:113], v[14:17]\n"
    "v_mfma_f32_16x16x32_bf16 v[18:21], v[86:89], v[114:117], v[18:21]\n"
    "v_mfma_f32_16x16x32_bf16 v[22:25], v[86:89], v[118:121], v[22:25]\n"
    "v_mfma_f32_16x16x32_bf16 v[94:97], v[82:85], v[142:145], 0\n"
    "v_mfma_f32_16x16x32_bf16 v[18:21], v[122:125], v[82:85], v[18:21]\n"
    "ds_read_b128 v[82:85], v154 offset:976\n"
    "s_waitcnt lgkmcnt(1)\n"
    "v_mfma_f32_16x16x32_bf16 v[98:101], v[74:77], v[138:141], 0\n"
    "v_cvt_pk_bf16_f32 v94, v90, v94\n"
    "v_cvt_pk_bf16_f32 v95, v91, v95\n"
    "v_mfma_f32_16x16x32_bf16 v[102:105], v[74:77], v[142:145], 0\n"
    "v_cvt_pk_bf16_f32 v96, v92, v96\n"
    "v_cvt_pk_bf16_f32 v97, v93, v97\n"
    "v_mfma_f32_16x16x32_bf16 v[22:25], v[122:125], v[74:77], v[22:25]\n"
    "ds_read_b128 v[74:77], v154 offset:1488\n"
    "v_mfma_f32_16x16x32_bf16 v[18:21], v[94:97], v[110:113], v[18:21]\n"
    "v_cvt_pk_bf16_f32 v102, v98, v102\n"
    "v_cvt_pk_bf16_f32 v103, v99, v103\n"
    "v_mfma_f32_16x16x32_bf16 v[22:25], v[94:97], v[114:117], v[22:25]\n"
    "v_cvt_pk_bf16_f32 v104, v100, v104\n"
    "v_cvt_pk_bf16_f32 v105, v101, v105\n"
    "v_mfma_f32_16x16x32_bf16 v[26:29], v[94:97], v[118:121], v[26:29]\n"
    "ds_read_b32 v166, v1 offset:4\n"
    "v_mfma_f32_16x16x32_bf16 v[22:25], v[102:105], v[110:113], v[22:25]\n"
    "v_mfma_f32_16x16x32_bf16 v[26:29], v[102:105], v[114:117], v[26:29]\n"
    "v_mfma_f32_16x16x32_bf16 v[30:33], v[102:105], v[118:121], v[30:33]\n"
    "v_mfma_f32_16x16x32_bf16 v[106:109], v[78:81], v[138:141], 0\n"
    "v_mfma_f32_16x16x32_bf16 v[86:89], v[78:81], v[142:145], 0\n"
    "ds_read_b128 v[78:81], v154 offset:2000\n"
    "v_mfma_f32_16x16x32_bf16 v[10:13], v[70:73], v[38:41], v[10:13]\n"
    "v_mfma_f32_16x16x32_bf16 v[14:17], v[58:61], v[38:41], v[14:17]\n"
    "v_cvt_pk_bf16_f32 v86, v106, v86\n"
    "s_waitcnt lgkmcnt(2)\n"
    "v_mfma_f32_16x16x32_bf16 v[90:93], v[82:85], v[138:141], 0\n"
    "v_cvt_pk_bf16_f32 v87, v107, v87\n"
    "v_cvt_pk_bf16_f32 v88, v108, v88\n"
    "v_mfma_f32_16x16x32_bf16 v[94:97], v[82:85], v[142:145], 0\n"
    "v_cvt_pk_bf16_f32 v89, v109, v89\n"
    "v_mfma_f32_16x16x32_bf16 v[98:101], v[74:77], v[138:141], 0\n"
    "s_waitcnt lgkmcnt(1)\n"
    "v_cmp_gt_u32 vcc, 4, v166\n"
    "s_cbranch_vccz .Lk62\n"
    "s_mov_b32 s46, 32768\n"
    ".Lk63:\n"
    "ds_read_b32 v166, v1 offset:4\n"
    "s_waitcnt lgkmcnt(0)\n"
    "v_cmp_gt_u32 vcc, 4, v166\n"
    "s_cbranch_vccz .Lk64\n"
    "s_sleep 1\n"
    "s_sub_u32 s46, s46, 1\n"
    "s_cbranch_scc0 .Lk63\n"
    ".Lk64:\n"
    ".Lk62:\n"
    "v_mfma_f32_16x16x32_bf16 v[10:13], v[86:89], v[126:129], v[10:13]\n"
    "ds_read_b128 v[46:49], v154 offset:2528\n"
    "ds_read_b128 v[50:53], v154 offset:3040\n"
    "v_mfma_f32_16x16x32_bf16 v[14:17], v[86:89], v[130:133], v[14:17]\n"
    "ds_read_b128 v[54:57], v154 offset:3552\n"
    "ds_read_b128 v[34:37], v154 offset:4064\n"
    "v_mfma_f32_16x16x32_bf16 v[18:21], v[86:89], v[134:137], v[18:21]\n"
    "ds_read_b128 v[38:41], v154 offset:2464\n"
    "v_cvt_pk_bf16_f32 v94, v90, v94\n"
    "v_mfma_f32_16x16x32_bf16 v[102:105], v[74:77], v[142:145], 0\n"
    "v_cvt_pk_bf16_f32 v95, v91, v95\n"
    "v_cvt_pk_bf16_f32 v96, v92, v96\n"
    "v_mfma_f32_16x16x32_bf16 v[14:17], v[70:73], v[42:45], v[14:17]\n"
    "ds_read_b128 v[82:85], v154 offset:2528\n"
    "v_cvt_pk_bf16_f32 v97, v93, v97\n"
    "v_mfma_f32_16x16x32_bf16 v[18:21], v[58:61], v[42:45], v[18:21]\n"
    "ds_read_b128 v[42:45], v154 offset:2976\n"
    "v_cvt_pk_bf16_f32 v102, v98, v102\n"
    "v_mfma_f32_16x16x32_bf16 v[14:17], v[94:97], v[126:129], v[14:17]\n"
    "v_cvt_pk_bf16_f32 v103, v99, v103\n"
    "v_cvt_pk_bf16_f32 v104, v100, v104\n"
    "v_mfma_f32_16x16x32_bf16 v[18:21], v[94:97], v[130:133], v[18:21]\n"
    "ds_read_b128 v[74:77], v154 offset:3040\n"
    "v_cvt_pk_bf16_f32 v105, v101, v105\n"
    "v_mfma_f32_16x16x32_bf16 v[22:25], v[94:97], v[134:137], v[22:25]\n"
    "s_waitcnt lgkmcnt(8)\n"
    "v_mfma_f32_16x16x32_bf16 v[106:109], v[78:81], v[138:141], 0\n"
    "v_mfma_f32_16x16x32_bf16 v[18:21], v[102:105], v[126:129], v[18:21]\n"
    "v_mfma_f32_16x16x32_bf16 v[22:25], v[102:105], v[130:133], v[22:25]\n"
    "v_mfma_f32_16x16x32_bf16 v[26:29], v[102:105], v[134:137], v[26:29]\n"
    "v_mfma_f32_16x16x32_bf16 v[86:89], v[78:81], v[142:145], 0\n"
    "ds_read_b128 v[78:81], v154 offset:2512\n"
    "s_waitcnt lgkmcnt(3)\n"
    "v_mfma_f32_16x16x32_bf16 v[18:21], v[62:65], v[46:49], v[18:21]\n"
    "v_mfma_f32_16x16x32_bf16 v[22:25], v[66:69], v[46:49], v[22:25]\n"
    "ds_read_b128 v[46:49], v154 offset:3488\n"
    "v_cvt_pk_bf16_f32 v86, v106, v86\n"
    "v_mfma_f32_16x16x32_bf16 v[22:25], v[62:65], v[50:53], v[22:25]\n"
    "v_cvt_pk_bf16_f32 v87, v107, v87\n"
    "v_cvt_pk_bf16_f32 v88, v108, v88\n"
    "v_mfma_f32_16x16x32_bf16 v[26:29], v[66:69], v[50:53], v[26:29]\n"
    "ds_read_b128 v[50:53], v154 offset:4000\n"
    "v_cvt_pk_bf16_f32 v89, v109, v89\n"
    "v_mfma_f32_16x16x32_bf16 v[26:29], v[62:65], v[54:57], v[26:29]\n"
    "v_mfma_f32_16x16x32_bf16 v[30:33], v[66:69], v[54:57], v[30:33]\n"
    "ds_read2_b32 v[66:67], v152 offset0:112 offset1:113\n"
    "ds_read2_b32 v[68:69], v152 offset0:114 offset1:115\n"
    "v_mfma_f32_16x16x32_bf16 v[30:33], v[62:65], v[34:37], v[30:33]\n"
    "ds_read2_b32 v[62:63], v153 offset0:80 offset1:81\n"
    "ds_read2_b32 v[64:65], v153 offset0:82 offset1:83\n"
    "v_mfma_f32_16x16x32_bf16 v[18:21], v[70:73], v[38:41], v[18:21]\n"
    "ds_read_b128 v[54:57], v154 offset:352\n"
    "ds_read_b128 v[34:37], v154 offset:864\n"
    "v_mfma_f32_16x16x32_bf16 v[22:25], v[58:61], v[38:41], v[22:25]\n"
    "ds_read_b128 v[38:41], v154 offset:1376\n"
    "s_waitcnt lgkmcnt(9)\n"
    "v_mfma_f32_16x16x32_bf16 v[22:25], v[70:73], v[42:45], v[22:25]\n"
    "v_mfma_f32_16x16x32_bf16 v[26:29], v[58:61], v[42:45], v[26:29]\n"
    "ds_read_b128 v[42:45], v154 offset:1888\n"
    "v_mfma_f32_16x16x32_bf16 v[22:25], v[86:89], v[126:129], v[22:25]\n"
    "v_mfma_f32_16x16x32_bf16 v[26:29], v[86:89], v[130:133], v[26:29]\n"
    "v_mfma_f32_16x16x32_bf16 v[30:33], v[86:89], v[134:137], v[30:33]\n"
    "v_mfma_f32_16x16x32_bf16 v[90:93], v[82:85], v[138:141], 0\n"
    "v_mfma_f32_16x16x32_bf16 v[94:97], v[82:85], v[142:145], 0\n"
    "v_mfma_f32_16x16x32_bf16 v[26:29], v[122:125], v[82:85], v[26:29]\n"
    "ds_read_b128 v[82:85], v154 offset:3024\n"
    "s_waitcnt lgkmcnt(3)\n"
    "v_mfma_f32_16x16x32_bf16 v[26:29], v[70:73], v[46:49], v[26:29]\n"
    "v_cvt_pk_bf16_f32 v94, v90, v94\n"
    "v_cvt_pk_bf16_f32 v95, v91, v95\n"
    "v_mfma_f32_16x16x32_bf16 v[30:33], v[58:61], v[46:49], v[30:33]\n"
    "ds_read_b128 v[46:49], v154 offset:2400\n"
    "v_cvt_pk_bf16_f32 v96, v92, v96\n"
    "v_mfma_f32_16x16x32_bf16 v[30:33], v[70:73], v[50:53], v[30:33]\n"
    "ds_read_b128 v[50:53], v154 offset:2912\n"
    "v_cvt_pk_bf16_f32 v97, v93, v97\n"
    "v_mfma_f32_16x16x32_bf16 v[2:5], v[62:65], v[54:57], v[2:5]\n"
    "ds_read2_b32 v[70:71], v153 offset0:64 offset1:65\n"
    "ds_read2_b32 v[72:73], v153 offset0:66 offset1:67\n"
    "v_mfma_f32_16x16x32_bf16 v[6:9], v[66:69], v[54:57], v[6:9]\n"
    "ds_read_b128 v[54:57], v154 offset:3424\n"
    "ds_read2_b32 v[58:59], v152 offset0:96 offset1:97\n"
    "v_mfma_f32_16x16x32_bf16 v[6:9], v[62:65], v[34:37], v[6:9]\n"
    "ds_read2_b32 v[60:61], v152 offset0:98 offset1:99\n"
    "v_mfma_f32_16x16x32_bf16 v[10:13], v[66:69], v[34:37], v[10:13]\n"
    "ds_read_b128 v[34:37], v154 offset:3936\n"
    "s_waitcnt lgkmcnt(9)\n"
    "v_mfma_f32_16x16x32_bf16 v[10:13], v[62:65], v[38:41], v[10:13]\n"
    "v_mfma_f32_16x16x32_bf16 v[14:17], v[66:69], v[38:41], v[14:17]\n"
    "ds_read_b128 v[38:41], v154 offset:288\n"
    "v_mfma_f32_16x16x32_bf16 v[14:17], v[62:65], v[42:45], v[14:17]\n"
    "v_mfma_f32_16x16x32_bf16 v[18:21], v[66:69], v[42:45], v[18:21]\n"
    "ds_read_b128 v[42:45], v154 offset:800\n"
    "s_waitcnt lgkmcnt(9)\n"
    "v_mfma_f32_16x16x32_bf16 v[18:21], v[62:65], v[46:49], v[18:21]\n"
    "v_mfma_f32_16x16x32_bf16 v[22:25], v[66:69], v[46:49], v[22:25]\n"
    "ds_read_b128 v[46:49], v154 offset:1312\n"
    "s_waitcnt lgkmcnt(7)\n"
    "v_mfma_f32_16x16x32_bf16 v[22:25], v[62:65], v[50:53], v[22:25]\n"
    "v_mfma_f32_16x16x32_bf16 v[26:29], v[66:69], v[50:53], v[26:29]\n"
    "ds_read_b128 v[50:53], v154 offset:1824\n"
    "s_waitcnt lgkmcnt(5)\n"
    "v_mfma_f32_16x16x32_bf16 v[26:29], v[62:65], v[54:57], v[26:29]\n"
    "v_mfma_f32_16x16x32_bf16 v[30:33], v[66:69], v[54:57], v[30:33]\n"
    "ds_read_b128 v[54:57], v154 offset:2336\n"
    "ds_read2_b32 v[66:67], v152 offset0:80 offset1:81\n"
    "v_mfma_f32_16x16x32_bf16 v[26:29], v[94:97], v[110:113], v[26:29]\n"
    "ds_read2_b32 v[68:69], v152 offset0:82 offset1:83\n"
    "v_mfma_f32_16x16x32_bf16 v[30:33], v[94:97], v[114:117], v[30:33]\n"
    "v_mfma_f32_16x16x32_bf16 v[98:101], v[74:77], v[138:141], 0\n"
    "v_mfma_f32_16x16x32_bf16 v[102:105], v[74:77], v[142:145], 0\n"
    "v_mfma_f32_16x16x32_bf16 v[30:33], v[122:125], v[74:77], v[30:33]\n"
    "s_waitcnt lgkmcnt(4)\n"
    "v_mfma_f32_16x16x32_bf16 v[30:33], v[62:65], v[34:37], v[30:33]\n"
    "ds_read_b128 v[34:37], v154 offset:2848\n"
    "ds_read2_b32 v[62:63], v153 offset0:48 offset1:49\n"
    "v_mfma_f32_16x16x32_bf16 v[2:5], v[70:73], v[38:41], v[2:5]\n"
    "ds_read2_b32 v[64:65], v153 offset0:50 offset1:51\n"
    "v_cvt_pk_bf16_f32 v102, v98, v102\n"
    "v_mfma_f32_16x16x32_bf16 v[6:9], v[58:61], v[38:41], v[6:9]\n"
    "ds_read_b128 v[38:41], v154 offset:3360\n"
    "v_cvt_pk_bf16_f32 v103, v99, v103\n"
    "v_mfma_f32_16x16x32_bf16 v[6:9], v[70:73], v[42:45], v[6:9]\n"
    "v_cvt_pk_bf16_f32 v104, v100, v104\n"
    "v_cvt_pk_bf16_f32 v105, v101, v105\n"
    "v_mfma_f32_16x16x32_bf16 v[10:13], v[58:61], v[42:45], v[10:13]\n"
    "ds_read_b128 v[42:45], v154 offset:3872\n"
    "v_mfma_f32_16x16x32_bf16 v[10:13], v[70:73], v[46:49], v[10:13]\n"
    "v_mfma_f32_16x16x32_bf16 v[14:17], v[58:61], v[46:49], v[14:17]\n"
    "ds_read_b128 v[46:49], v154 offset:224\n"
    "s_waitcnt lgkmcnt(6)\n"
    "v_mfma_f32_16x16x32_bf16 v[14:17], v[70:73], v[50:53], v[14:17]\n"
    "v_mfma_f32_16x16x32_bf16 v[18:21], v[58:61], v[50:53], v[18:21]\n"
    "ds_read_b128 v[50:53], v154 offset:736\n"
    "v_mfma_f32_16x16x32_bf16 v[18:21], v[70:73], v[54:57], v[18:21]\n"
    "v_mfma_f32_16x16x32_bf16 v[22:25], v[58:61], v[54:57], v[22:25]\n"
    "ds_read_b128 v[54:57], v154 offset:1248\n"
    "s_waitcnt lgkmcnt(5)\n"
    "v_mfma_f32_16x16x32_bf16 v[22:25], v[70:73], v[34:37], v[22:25]\n"
    "v_mfma_f32_16x16x32_bf16 v[26:29], v[58:61], v[34:37], v[26:29]\n"
    "ds_read_b128 v[34:37], v154 offset:1760\n"
    "s_waitcnt lgkmcnt(5)\n"
    "v_mfma_f32_16x16x32_bf16 v[26:29], v[70:73], v[38:41], v[26:29]\n"
    "v_mfma_f32_16x16x32_bf16 v[30:33], v[58:61], v[38:41], v[30:33]\n"
    "ds_read_b128 v[38:41], v154 offset:2272\n"
    "s_waitcnt lgkmcnt(5)\n"
    "v_mfma_f32_16x16x32_bf16 v[30:33], v[70:73], v[42:45], v[30:33]\n"
    "ds_read_b128 v[42:45], v154 offset:2784\n"
    "s_waitcnt lgkmcnt(5)\n"
    "v_mfma_f32_16x16x32_bf16 v[2:5], v[62:65], v[46:49], v[2:5]\n"
    "ds_read2_b32 v[70:71], v153 offset0:32 offset1:33\n"
    "ds_read2_b32 v[72:73], v153 offset0:34 offset1:35\n"
    "v_mfma_f32_16x16x32_bf16 v[6:9], v[66:69], v[46:49], v[6:9]\n"
    "ds_read_b128 v[46:49], v154 offset:3296\n"
    "ds_read2_b32 v[58:59], v152 offset0:64 offset1:65\n"
    "v_mfma_f32_16x16x32_bf16 v[30:33], v[102:105], v[110:113], v[30:33]\n"
    "ds_read2_b32 v[60:61], v152 offset0:66 offset1:67\n"
    "v_mfma_f32_16x16x32_bf16 v[106:109], v[78:81], v[138:141], 0\n"
    "v_mfma_f32_16x16x32_bf16 v[86:89], v[78:81], v[142:145], 0\n"
    "s_waitcnt lgkmcnt(8)\n"
    "v_mfma_f32_16x16x32_bf16 v[6:9], v[62:65], v[50:53], v[6:9]\n"
    "v_mfma_f32_16x16x32_bf16 v[10:13], v[66:69], v[50:53], v[10:13]\n"
    "ds_read_b128 v[50:53], v154 offset:3808\n"
    "v_cvt_pk_bf16_f32 v86, v106, v86\n"
    "v_mfma_f32_16x16x32_bf16 v[10:13], v[62:65], v[54:57], v[10:13]\n"
    "v_cvt_pk_bf16_f32 v87, v107, v87\n"
    "v_cvt_pk_bf16_f32 v88, v108, v88\n"
    "v_mfma_f32_16x16x32_bf16 v[14:17], v[66:69], v[54:57], v[14:17]\n"
    "ds_read_b128 v[54:57], v154 offset:160\n"
    "s_waitcnt lgkmcnt(7)\n"
    "v_mfma_f32_16x16x32_bf16 v[14:17], v[62:65], v[34:37], v[14:17]\n"
    "v_cvt_pk_bf16_f32 v89, v109, v89\n"
    "v_mfma_f32_16x16x32_bf16 v[18:21], v[66:69], v[34:37], v[18:21]\n"
    "ds_read_b128 v[34:37], v154 offset:672\n"
    "v_mfma_f32_16x16x32_bf16 v[18:21], v[62:65], v[38:41], v[18:21]\n"
    "v_mfma_f32_16x16x32_bf16 v[22:25], v[66:69], v[38:41], v[22:25]\n"
    "ds_read_b128 v[38:41], v154 offset:1184\n"
    "v_mfma_f32_16x16x32_bf16 v[22:25], v[62:65], v[42:45], v[22:25]\n"
    "v_mfma_f32_16x16x32_bf16 v[26:29], v[66:69], v[42:45], v[26:29]\n"
    "ds_read_b128 v[42:45], v154 offset:1696\n"
    "s_waitcnt lgkmcnt(5)\n"
    "v_mfma_f32_16x16x32_bf16 v[26:29], v[62:65], v[46:49], v[26:29]\n"
    "v_mfma_f32_16x16x32_bf16 v[30:33], v[66:69], v[46:49], v[30:33]\n"
    "ds_read_b128 v[46:49], v154 offset:2208\n"
    "s_waitcnt lgkmcnt(5)\n"
    "v_mfma_f32_16x16x32_bf16 v[30:33], v[62:65], v[50:53], v[30:33]\n"
    "ds_read_b128 v[50:53], v154 offset:2720\n"
    "s_waitcnt lgkmcnt(5)\n"
    "v_mfma_f32_16x16x32_bf16 v[2:5], v[70:73], v[54:57], v[2:5]\n"
    "ds_read2_b32 v[62:63], v153 offset0:16 offset1:17\n"
    "ds_read2_b32 v[64:65], v153 offset0:18 offset1:19\n"
    "v_mfma_f32_16x16x32_bf16 v[6:9], v[58:61], v[54:57], v[6:9]\n"
    "ds_read_b128 v[54:57], v154 offset:3232\n"
    "s_waitcnt lgkmcnt(7)\n"
    "v_mfma_f32_16x16x32_bf16 v[6:9], v[70:73], v[34:37], v[6:9]\n"
    "ds_read2_b32 v[66:67], v152 offset0:48 offset1:49\n"
    "ds_read2_b32 v[68:69], v152 offset0:50 offset1:51\n"
    "v_mfma_f32_16x16x32_bf16 v[10:13], v[58:61], v[34:37], v[10:13]\n"
    "ds_read_b128 v[34:37], v154 offset:3744\n"
    "v_mfma_f32_16x16x32_bf16 v[26:29], v[86:89], v[126:129], v[26:29]\n"
    "v_mfma_f32_16x16x32_bf16 v[30:33], v[86:89], v[130:133], v[30:33]\n"
    "v_mfma_f32_16x16x32_bf16 v[90:93], v[82:85], v[138:141], 0\n"
    "v_mfma_f32_16x16x32_bf16 v[94:97], v[82:85], v[142:145], 0\n"
    "s_waitcnt lgkmcnt(7)\n"
    "v_mfma_f32_16x16x32_bf16 v[10:13], v[70:73], v[38:41], v[10:13]\n"
    "v_mfma_f32_16x16x32_bf16 v[14:17], v[58:61], v[38:41], v[14:17]\n"
    "ds_read_b128 v[38:41], v154 offset:96\n"
    "v_cvt_pk_bf16_f32 v94, v90, v94\n"
    "v_mfma_f32_16x16x32_bf16 v[14:17], v[70:73], v[42:45], v[14:17]\n"
    "s_waitcnt lgkmcnt(4)\n"
    "ds_read_b32 v164, v1 offset:8\n"
    "v_mfma_f32_16x16x32_bf16 v[18:21], v[58:61], v[42:45], v[18:21]\n"
    "ds_read_b128 v[42:45], v154 offset:608\n"
    "v_cvt_pk_bf16_f32 v95, v91, v95\n"
    "v_mfma_f32_16x16x32_bf16 v[18:21], v[70:73], v[46:49], v[18:21]\n"
    "v_cvt_pk_bf16_f32 v96, v92, v96\n"
    "v_cvt_pk_bf16_f32 v97, v93, v97\n"
    "v_mfma_f32_16x16x32_bf16 v[22:25], v[58:61], v[46:49], v[22:25]\n"
    "ds_read_b128 v[46:49], v154 offset:1120\n"
    "v_mfma_f32_16x16x32_bf16 v[22:25], v[70:73], v[50:53], v[22:25]\n"
    "v_mfma_f32_16x16x32_bf16 v[26:29], v[58:61], v[50:53], v[26:29]\n"
    "ds_read_b128 v[50:53], v154 offset:1632\n"
    "v_mfma_f32_16x16x32_bf16 v[26:29], v[70:73], v[54:57], v[26:29]\n"
    "v_mfma_f32_16x16x32_bf16 v[30:33], v[58:61], v[54:57], v[30:33]\n"
    "ds_read_b128 v[54:57], v154 offset:2144\n"
    "s_waitcnt lgkmcnt(6)\n"
    "v_mfma_f32_16x16x32_bf16 v[30:33], v[70:73], v[34:37], v[30:33]\n"
    "ds_read_b128 v[34:37], v154 offset:2656\n"
    "s_waitcnt lgkmcnt(6)\n"
    "v_mfma_f32_16x16x32_bf16 v[2:5], v[62:65], v[38:41], v[2:5]\n"
    "ds_read2_b32 v[70:71], v153 offset0:0 offset1:1\n"
    "ds_read2_b32 v[72:73], v153 offset0:2 offset1:3\n"
    "v_mfma_f32_16x16x32_bf16 v[6:9], v[66:69], v[38:41], v[6:9]\n"
    "ds_read_b128 v[38:41], v154 offset:3168\n"
    "s_waitcnt lgkmcnt(7)\n"
    "v_mfma_f32_16x16x32_bf16 v[6:9], v[62:65], v[42:45], v[6:9]\n"
    "ds_read2_b32 v[58:59], v152 offset0:32 offset1:33\n"
    "ds_read2_b32 v[60:61], v152 offset0:34 offset1:35\n"
    "v_mfma_f32_16x16x32_bf16 v[10:13], v[66:69], v[42:45], v[10:13]\n"
    "ds_read_b128 v[42:45], v154 offset:3680\n"
    "s_waitcnt lgkmcnt(9)\n"
    "v_mfma_f32_16x16x32_bf16 v[10:13], v[62:65], v[46:49], v[10:13]\n"
    "v_mfma_f32_16x16x32_bf16 v[14:17], v[66:69], v[46:49], v[14:17]\n"
    "ds_read_b128 v[46:49], v154 offset:32\n"
    "s_waitcnt lgkmcnt(9)\n"
    "v_mfma_f32_16x16x32_bf16 v[14:17], v[62:65], v[50:53], v[14:17]\n"
    "v_mfma_f32_16x16x32_bf16 v[18:21], v[66:69], v[50:53], v[18:21]\n"
    "ds_read_b128 v[50:53], v154 offset:544\n"
    "s_waitcnt lgkmcnt(9)\n"
    "v_mfma_f32_16x16x32_bf16 v[18:21], v[62:65], v[54:57], v[18:21]\n"
    "v_mfma_f32_16x16x32_bf16 v[22:25], v[66:69], v[54:57], v[22:25]\n"
    "ds_read_b128 v[54:57], v154 offset:1056\n"
    "s_waitcnt lgkmcnt(7)\n"
    "v_mfma_f32_16x16x32_bf16 v[22:25], v[62:65], v[34:37], v[22:25]\n"
    "v_mfma_f32_16x16x32_bf16 v[26:29], v[66:69], v[34:37], v[26:29]\n"
    "ds_read_b128 v[34:37], v154 offset:1568\n"
    "s_waitcnt lgkmcnt(5)\n"
    "v_mfma_f32_16x16x32_bf16 v[26:29], v[62:65], v[38:41], v[26:29]\n"
    "v_mfma_f32_16x16x32_bf16 v[30:33], v[66:69], v[38:41], v[30:33]\n"
    "ds_read_b128 v[38:41], v154 offset:2080\n"
    "s_waitcnt lgkmcnt(5)\n"
    "v_mfma_f32_16x16x32_bf16 v[30:33], v[62:65], v[42:45], v[30:33]\n"
    "ds_read_b128 v[42:45], v154 offset:2592\n"
    "v_mfma_f32_16x16x32_bf16 v[30:33], v[94:97], v[126:129], v[30:33]\n"
    "s_waitcnt lgkmcnt(5)\n"
    "v_mfma_f32_16x16x32_bf16 v[2:5], v[70:73], v[46:49], v[2:5]\n"
    "v_mfma_f32_16x16x32_bf16 v[6:9], v[58:61], v[46:49], v[6:9]\n"
    "ds_read_b128 v[46:49], v154 offset:3104\n"
    "s_waitcnt lgkmcnt(5)\n"
    "v_mfma_f32_16x16x32_bf16 v[6:9], v[70:73], v[50:53], v[6:9]\n"
    "v_cvt_pk_bf16_f32 v2, v2, v3\n"
    "v_cvt_pk_bf16_f32 v4, v4, v5\n"
    "v_mfma_f32_16x16x32_bf16 v[10:13], v[58:61], v[50:53], v[10:13]\n"
    "v_cmp_gt_u32 vcc, 4, v164\n"
    "s_cbranch_vccz .Lk65\n"
    "s_mov_b32 s46, 32768\n"
    ".Lk66:\n"
    "ds_read_b32 v164, v1 offset:8\n"
    "s_waitcnt lgkmcnt(0)\n"
    "v_cmp_gt_u32 vcc, 4, v164\n"
    "s_cbranch_vccz .Lk67\n"
    "s_sleep 1\n"
    "s_sub_u32 s46, s46, 1\n"
    "s_cbranch_scc0 .Lk66\n"
    ".Lk67:\n"
    ".Lk65:\n"
    "buffer_store_dword v2, v157, s[16:19], s62 offen offset:0 sc1\n"
    "s_waitcnt lgkmcnt(4)\n"
    "v_mfma_f32_16x16x32_bf16 v[10:13], v[70:73], v[54:57], v[10:13]\n"
    "buffer_store_dword v4, v157, s[16:19], s62 offen offset:256 sc1\n"
    "ds_read_b128 v[50:53], v154 offset:3616\n"
    "v_mfma_f32_16x16x32_bf16 v[14:17], v[58:61], v[54:57], v[14:17]\n"
    "v_cvt_pk_bf16_f32 v6, v6, v7\n"
    "s_waitcnt lgkmcnt(4)\n"
    "v_mfma_f32_16x16x32_bf16 v[14:17], v[70:73], v[34:37], v[14:17]\n"
    "buffer_store_dword v6, v157, s[16:19], s62 offen offset:512 sc1\n"
    "v_cvt_pk_bf16_f32 v8, v8, v9\n"
    "v_mfma_f32_16x16x32_bf16 v[18:21], v[58:61], v[34:37], v[18:21]\n"
    "buffer_store_dword v8, v157, s[16:19], s62 offen offset:768 sc1\n"
    "s_waitcnt lgkmcnt(2)\n"
    "v_mfma_f32_16x16x32_bf16 v[18:21], v[70:73], v[38:41], v[18:21]\n"
    "v_cvt_pk_bf16_f32 v10, v10, v11\n"
    "v_cvt_pk_bf16_f32 v12, v12, v13\n"
    "v_mfma_f32_16x16x32_bf16 v[22:25], v[58:61], v[38:41], v[22:25]\n"
    "buffer_store_dword v10, v157, s[16:19], s62 offen offset:1024 sc1\n"
    "buffer_store_dword v12, v157, s[16:19], s62 offen offset:1280 sc1\n"
    "v_mfma_f32_16x16x32_bf16 v[22:25], v[70:73], v[42:45], v[22:25]\n"
    "v_cvt_pk_bf16_f32 v14, v14, v15\n"
    "v_cvt_pk_bf16_f32 v16, v16, v17\n"
    "v_mfma_f32_16x16x32_bf16 v[26:29], v[58:61], v[42:45], v[26:29]\n"
    "buffer_store_dword v14, v157, s[16:19], s62 offen offset:1536 sc1\n"
    "s_waitcnt lgkmcnt(1)\n"
    "v_mfma_f32_16x16x32_bf16 v[26:29], v[70:73], v[46:49], v[26:29]\n"
    "buffer_store_dword v16, v157, s[16:19], s62 offen offset:1792 sc1\n"
    "v_cvt_pk_bf16_f32 v18, v18, v19\n"
    "v_mfma_f32_16x16x32_bf16 v[30:33], v[58:61], v[46:49], v[30:33]\n"
    "buffer_store_dword v18, v157, s[16:19], s62 offen offset:2048 sc1\n"
    "s_waitcnt lgkmcnt(0)\n"
    "v_mfma_f32_16x16x32_bf16 v[30:33], v[70:73], v[50:53], v[30:33]\n"
    "v_cvt_pk_bf16_f32 v20, v20, v21\n"
    "v_cvt_pk_bf16_f32 v22, v22, v23\n"
    "v_cvt_pk_bf16_f32 v24, v24, v25\n"
    "v_cvt_pk_bf16_f32 v26, v26, v27\n"
    "v_cvt_pk_bf16_f32 v28, v28, v29\n"
    "buffer_store_dword v20, v157, s[16:19], s62 offen offset:2304 sc1\n"
    "buffer_store_dword v22, v157, s[16:19], s62 offen offset:2560 sc1\n"
    "buffer_store_dword v24, v157, s[16:19], s62 offen offset:2816 sc1\n"
    "buffer_store_dword v26, v157, s[16:19], s62 offen offset:3072 sc1\n"
    "buffer_store_dword v28, v157, s[16:19], s62 offen offset:3328 sc1\n"
    "v_cvt_pk_bf16_f32 v30, v30, v31\n"
    "v_cvt_pk_bf16_f32 v32, v32, v33\n"
    "buffer_store_dword v30, v157, s[16:19], s62 offen offset:3584 sc1\n"
    "buffer_store_dword v32, v157, s[16:19], s62 offen offset:3840 sc1\n"
    "s_branch .Lk56\n"
    ".Lk57:\n"
    "s_mov_b32 s46, 32768\n"
    ".Lk68:\n"
    "ds_read_b32 v165, v1 offset:4\n"
    "s_waitcnt lgkmcnt(0)\n"
    "v_cmp_gt_u32 vcc, 4, v165\n"
    "s_cbranch_vccz .Lk69\n"
    "s_sleep 1\n"
    "s_sub_u32 s46, s46, 1\n"
    "s_cbranch_scc0 .Lk68\n"
    ".Lk69:\n"
    "s_and_b32 s53, s44, 1\n"
    "s_cbranch_scc0 .Lk58\n"
    "v_lshlrev_b32 v165, 2, v157\n"
    "v_add_u32 v165, 145280, v165\n"
    "ds_read_b128 v[134:137], v165 offset:0\n"
    "ds_read_b128 v[138:141], v165 offset:1024\n"
    "ds_read_b128 v[142:145], v165 offset:2048\n"
    "ds_read_b128 v[146:149], v165 offset:3072\n"
    "ds_read2_b32 v[126:127], v152 offset0:16 offset1:17\n"
    "ds_read2_b32 v[128:129], v152 offset0:18 offset1:19\n"
    "ds_read2_b32 v[130:131], v152 offset0:0 offset1:1\n"
    "ds_read2_b32 v[132:133], v152 offset0:2 offset1:3\n"
    "ds_read2_b32 v[98:99], v151 offset0:48 offset1:49\n"
    "ds_read2_b32 v[100:101], v151 offset0:50 offset1:51\n"
    "ds_read2_b32 v[102:103], v151 offset0:32 offset1:33\n"
    "ds_read2_b32 v[104:105], v151 offset0:34 offset1:35\n"
    "ds_read2_b32 v[106:107], v151 offset0:16 offset1:17\n"
    "ds_read2_b32 v[108:109], v151 offset0:18 offset1:19\n"
    "ds_read2_b32 v[110:111], v151 offset0:96 offset1:97\n"
    "ds_read2_b32 v[112:113], v151 offset0:98 offset1:99\n"
    "ds_read2_b32 v[114:115], v151 offset0:80 offset1:81\n"
    "ds_read2_b32 v[116:117], v151 offset0:82 offset1:83\n"
    "ds_read2_b32 v[118:119], v151 offset0:64 offset1:65\n"
    "ds_read2_b32 v[120:121], v151 offset0:66 offset1:67\n"
    "ds_read2_b32 v[122:123], v151 offset0:175 offset1:176\n"
    "ds_read2_b32 v[124:125], v151 offset0:177 offset1:178\n"
    "s_waitcnt lgkmcnt(0)\n"
    "s_nop 0\n"
    "ds_read2_b32 v[50:51], v153 offset0:112 offset1:113\n"
    "ds_read2_b32 v[52:53], v153 offset0:114 offset1:115\n"
    "ds_read2_b32 v[54:55], v152 offset0:144 offset1:145\n"
    "ds_read2_b32 v[56:57], v152 offset0:146 offset1:147\n"
    "ds_read_b128 v[26:29], v154 offset:480\n"
    "ds_read_b128 v[30:33], v154 offset:992\n"
    "ds_read_b128 v[34:37], v154 offset:1504\n"
    "ds_read_b128 v[38:41], v154 offset:2016\n"
    "ds_read_b128 v[42:45], v154 offset:2528\n"
    "ds_read_b128 v[66:69], v154 offset:480\n"
    "ds_read2_b32 v[58:59], v153 offset0:96 offset1:97\n"
    "ds_read2_b32 v[60:61], v153 offset0:98 offset1:99\n"
    "ds_read2_b32 v[46:47], v152 offset0:128 offset1:129\n"
    "ds_read2_b32 v[48:49], v152 offset0:130 offset1:131\n"
    "ds_read_b128 v[22:25], v154 offset:416\n"
    "ds_read_b128 v[70:73], v154 offset:992\n"
    "s_waitcnt lgkmcnt(11)\n"
    "v_mfma_f32_16x16x32_bf16 v[2:5], v[50:53], v[26:29], 0\n"
    "ds_read_b128 v[62:65], v154 offset:1504\n"
    "v_mfma_f32_16x16x32_bf16 v[6:9], v[54:57], v[26:29], 0\n"
    "ds_read_b128 v[26:29], v154 offset:928\n"
    "s_waitcnt lgkmcnt(6)\n"
    "v_mfma_f32_16x16x32_bf16 v[6:9], v[50:53], v[30:33], v[6:9]\n"
    "v_mfma_f32_16x16x32_bf16 v[10:13], v[54:57], v[30:33], 0\n"
    "ds_read_b128 v[30:33], v154 offset:1440\n"
    "v_mfma_f32_16x16x32_bf16 v[10:13], v[50:53], v[34:37], v[10:13]\n"
    "v_mfma_f32_16x16x32_bf16 v[14:17], v[54:57], v[34:37], 0\n"
    "ds_read_b128 v[34:37], v154 offset:1952\n"
    "v_mfma_f32_16x16x32_bf16 v[14:17], v[50:53], v[38:41], v[14:17]\n"
    "v_mfma_f32_16x16x32_bf16 v[18:21], v[54:57], v[38:41], 0\n"
    "ds_read_b128 v[38:41], v154 offset:2464\n"
    "ds_read2_b32 v[54:55], v152 offset0:112 offset1:113\n"
    "v_mfma_f32_16x16x32_bf16 v[18:21], v[50:53], v[42:45], v[18:21]\n"
    "ds_read2_b32 v[50:51], v153 offset0:80 offset1:81\n"
    "ds_read2_b32 v[52:53], v153 offset0:82 offset1:83\n"
    "v_mfma_f32_16x16x32_bf16 v[78:81], v[66:69], v[134:137], 0\n"
    "ds_read2_b32 v[56:57], v152 offset0:114 offset1:115\n"
    "ds_read_b128 v[42:45], v154 offset:352\n"
    "v_mfma_f32_16x16x32_bf16 v[82:85], v[66:69], v[138:141], 0\n"
    "v_mfma_f32_16x16x32_bf16 v[10:13], v[126:129], v[66:69], v[10:13]\n"
    "ds_read_b128 v[66:69], v154 offset:464\n"
    "s_waitcnt lgkmcnt(9)\n"
    "v_mfma_f32_16x16x32_bf16 v[2:5], v[58:61], v[22:25], v[2:5]\n"
    "v_cvt_pk_bf16_f32 v82, v78, v82\n"
    "v_cvt_pk_bf16_f32 v83, v79, v83\n"
    "v_mfma_f32_16x16x32_bf16 v[6:9], v[46:49], v[22:25], v[6:9]\n"
    "ds_read_b128 v[22:25], v154 offset:864\n"
    "v_cvt_pk_bf16_f32 v84, v80, v84\n"
    "v_mfma_f32_16x16x32_bf16 v[6:9], v[58:61], v[26:29], v[6:9]\n"
    "v_cvt_pk_bf16_f32 v85, v81, v85\n"
    "v_mfma_f32_16x16x32_bf16 v[10:13], v[46:49], v[26:29], v[10:13]\n"
    "ds_read_b128 v[26:29], v154 offset:1376\n"
    "s_waitcnt lgkmcnt(9)\n"
    "v_mfma_f32_16x16x32_bf16 v[10:13], v[58:61], v[30:33], v[10:13]\n"
    "v_mfma_f32_16x16x32_bf16 v[14:17], v[46:49], v[30:33], v[14:17]\n"
    "ds_read_b128 v[30:33], v154 offset:1888\n"
    "v_mfma_f32_16x16x32_bf16 v[14:17], v[58:61], v[34:37], v[14:17]\n"
    "v_mfma_f32_16x16x32_bf16 v[18:21], v[46:49], v[34:37], v[18:21]\n"
    "ds_read_b128 v[34:37], v154 offset:2400\n"
    "ds_read2_b32 v[46:47], v152 offset0:96 offset1:97\n"
    "v_mfma_f32_16x16x32_bf16 v[10:13], v[82:85], v[98:101], v[10:13]\n"
    "ds_read2_b32 v[48:49], v152 offset0:98 offset1:99\n"
    "v_mfma_f32_16x16x32_bf16 v[14:17], v[82:85], v[102:105], v[14:17]\n"
    "v_mfma_f32_16x16x32_bf16 v[18:21], v[82:85], v[106:109], v[18:21]\n"
    "v_mfma_f32_16x16x32_bf16 v[86:89], v[70:73], v[134:137], 0\n"
    "v_mfma_f32_16x16x32_bf16 v[90:93], v[70:73], v[138:141], 0\n"
    "v_mfma_f32_16x16x32_bf16 v[14:17], v[126:129], v[70:73], v[14:17]\n"
    "ds_read_b128 v[70:73], v154 offset:976\n"
    "s_waitcnt lgkmcnt(5)\n"
    "v_mfma_f32_16x16x32_bf16 v[18:21], v[58:61], v[38:41], v[18:21]\n"
    "ds_read2_b32 v[58:59], v153 offset0:64 offset1:65\n"
    "ds_read2_b32 v[60:61], v153 offset0:66 offset1:67\n"
    "v_mfma_f32_16x16x32_bf16 v[2:5], v[50:53], v[42:45], v[2:5]\n"
    "ds_read_b128 v[38:41], v154 offset:288\n"
    "v_cvt_pk_bf16_f32 v90, v86, v90\n"
    "v_mfma_f32_16x16x32_bf16 v[6:9], v[54:57], v[42:45], v[6:9]\n"
    "ds_read_b128 v[42:45], v154 offset:800\n"
    "v_cvt_pk_bf16_f32 v91, v87, v91\n"
    "v_mfma_f32_16x16x32_bf16 v[6:9], v[50:53], v[22:25], v[6:9]\n"
    "v_cvt_pk_bf16_f32 v92, v88, v92\n"
    "v_cvt_pk_bf16_f32 v93, v89, v93\n"
    "v_mfma_f32_16x16x32_bf16 v[10:13], v[54:57], v[22:25], v[10:13]\n"
    "ds_read_b128 v[22:25], v154 offset:1312\n"
    "v_mfma_f32_16x16x32_bf16 v[10:13], v[50:53], v[26:29], v[10:13]\n"
    "v_mfma_f32_16x16x32_bf16 v[14:17], v[54:57], v[26:29], v[14:17]\n"
    "ds_read_b128 v[26:29], v154 offset:1824\n"
    "v_mfma_f32_16x16x32_bf16 v[14:17], v[90:93], v[98:101], v[14:17]\n"
    "v_mfma_f32_16x16x32_bf16 v[18:21], v[90:93], v[102:105], v[18:21]\n"
    "v_mfma_f32_16x16x32_bf16 v[94:97], v[62:65], v[134:137], 0\n"
    "v_mfma_f32_16x16x32_bf16 v[74:77], v[62:65], v[138:141], 0\n"
    "v_mfma_f32_16x16x32_bf16 v[18:21], v[126:129], v[62:65], v[18:21]\n"
    "ds_read_b128 v[62:65], v154 offset:1488\n"
    "s_waitcnt lgkmcnt(3)\n"
    "v_mfma_f32_16x16x32_bf16 v[14:17], v[50:53], v[30:33], v[14:17]\n"
    "v_cvt_pk_bf16_f32 v74, v94, v74\n"
    "v_cvt_pk_bf16_f32 v75, v95, v75\n"
    "v_mfma_f32_16x16x32_bf16 v[18:21], v[54:57], v[30:33], v[18:21]\n"
    "ds_read_b128 v[30:33], v154 offset:2336\n"
    "ds_read2_b32 v[54:55], v152 offset0:80 offset1:81\n"
    "v_mfma_f32_16x16x32_bf16 v[18:21], v[50:53], v[34:37], v[18:21]\n"
    "ds_read2_b32 v[50:51], v153 offset0:48 offset1:49\n"
    "ds_read2_b32 v[52:53], v153 offset0:50 offset1:51\n"
    "v_mfma_f32_16x16x32_bf16 v[2:5], v[58:61], v[38:41], v[2:5]\n"
    "ds_read2_b32 v[56:57], v152 offset0:82 offset1:83\n"
    "ds_read_b128 v[34:37], v154 offset:224\n"
    "v_mfma_f32_16x16x32_bf16 v[6:9], v[46:49], v[38:41], v[6:9]\n"
    "ds_read_b128 v[38:41], v154 offset:736\n"
    "v_cvt_pk_bf16_f32 v76, v96, v76\n"
    "v_mfma_f32_16x16x32_bf16 v[6:9], v[58:61], v[42:45], v[6:9]\n"
    "v_cvt_pk_bf16_f32 v77, v97, v77\n"
    "v_mfma_f32_16x16x32_bf16 v[10:13], v[46:49], v[42:45], v[10:13]\n"
    "ds_read_b128 v[42:45], v154 offset:1248\n"
    "v_mfma_f32_16x16x32_bf16 v[18:21], v[74:77], v[98:101], v[18:21]\n"
    "v_mfma_f32_16x16x32_bf16 v[78:81], v[66:69], v[134:137], 0\n"
    "v_mfma_f32_16x16x32_bf16 v[82:85], v[66:69], v[138:141], 0\n"
    "ds_read_b128 v[66:69], v154 offset:480\n"
    "s_waitcnt lgkmcnt(9)\n"
    "v_mfma_f32_16x16x32_bf16 v[10:13], v[58:61], v[22:25], v[10:13]\n"
    "v_mfma_f32_16x16x32_bf16 v[14:17], v[46:49], v[22:25], v[14:17]\n"
    "ds_read_b128 v[22:25], v154 offset:1760\n"
    "v_cvt_pk_bf16_f32 v82, v78, v82\n"
    "v_mfma_f32_16x16x32_bf16 v[14:17], v[58:61], v[26:29], v[14:17]\n"
    "v_cvt_pk_bf16_f32 v83, v79, v83\n"
    "v_cvt_pk_bf16_f32 v84, v80, v84\n"
    "v_mfma_f32_16x16x32_bf16 v[18:21], v[46:49], v[26:29], v[18:21]\n"
    "ds_read_b128 v[26:29], v154 offset:2272\n"
    "s_waitcnt lgkmcnt(4)\n"
    "v_mfma_f32_16x16x32_bf16 v[18:21], v[58:61], v[30:33], v[18:21]\n"
    "ds_read2_b32 v[58:59], v153 offset0:32 offset1:33\n"
    "ds_read2_b32 v[60:61], v153 offset0:34 offset1:35\n"
    "v_mfma_f32_16x16x32_bf16 v[2:5], v[50:53], v[34:37], v[2:5]\n"
    "ds_read2_b32 v[46:47], v152 offset0:64 offset1:65\n"
    "v_cvt_pk_bf16_f32 v85, v81, v85\n"
    "v_mfma_f32_16x16x32_bf16 v[6:9], v[54:57], v[34:37], v[6:9]\n"
    "ds_read2_b32 v[48:49], v152 offset0:66 offset1:67\n"
    "ds_read_b128 v[30:33], v154 offset:160\n"
    "v_mfma_f32_16x16x32_bf16 v[6:9], v[50:53], v[38:41], v[6:9]\n"
    "ds_read_b128 v[34:37], v154 offset:672\n"
    "v_mfma_f32_16x16x32_bf16 v[10:13], v[54:57], v[38:41], v[10:13]\n"
    "ds_read_b128 v[38:41], v154 offset:1184\n"
    "v_mfma_f32_16x16x32_bf16 v[10:13], v[82:85], v[110:113], v[10:13]\n"
    "v_mfma_f32_16x16x32_bf16 v[14:17], v[82:85], v[114:117], v[14:17]\n"
    "v_mfma_f32_16x16x32_bf16 v[18:21], v[82:85], v[118:121], v[18:21]\n"
    "v_mfma_f32_16x16x32_bf16 v[86:89], v[70:73], v[134:137], 0\n"
    "v_mfma_f32_16x16x32_bf16 v[90:93], v[70:73], v[138:141], 0\n"
    "ds_read_b128 v[70:73], v154 offset:464\n"
    "s_waitcnt lgkmcnt(6)\n"
    "v_mfma_f32_16x16x32_bf16 v[10:13], v[50:53], v[42:45], v[10:13]\n"
    "ds_read_b32 v166, v1 offset:8\n"
    "v_mfma_f32_16x16x32_bf16 v[14:17], v[54:57], v[42:45], v[14:17]\n"
    "ds_read_b128 v[42:45], v154 offset:1696\n"
    "v_cvt_pk_bf16_f32 v90, v86, v90\n"
    "v_mfma_f32_16x16x32_bf16 v[14:17], v[50:53], v[22:25], v[14:17]\n"
    "v_cvt_pk_bf16_f32 v91, v87, v91\n"
    "v_cvt_pk_bf16_f32 v92, v88, v92\n"
    "v_mfma_f32_16x16x32_bf16 v[18:21], v[54:57], v[22:25], v[18:21]\n"
    "ds_read_b128 v[22:25], v154 offset:2208\n"
    "v_cvt_pk_bf16_f32 v93, v89, v93\n"
    "v_mfma_f32_16x16x32_bf16 v[18:21], v[50:53], v[26:29], v[18:21]\n"
    "ds_read2_b32 v[50:51], v153 offset0:16 offset1:17\n"
    "s_waitcnt lgkmcnt(5)\n"
    "v_mfma_f32_16x16x32_bf16 v[2:5], v[58:61], v[30:33], v[2:5]\n"
    "ds_read2_b32 v[52:53], v153 offset0:18 offset1:19\n"
    "ds_read2_b32 v[54:55], v152 offset0:48 offset1:49\n"
    "v_mfma_f32_16x16x32_bf16 v[6:9], v[46:49], v[30:33], v[6:9]\n"
    "ds_read2_b32 v[56:57], v152 offset0:50 offset1:51\n"
    "ds_read_b128 v[26:29], v154 offset:96\n"
    "v_mfma_f32_16x16x32_bf16 v[14:17], v[90:93], v[110:113], v[14:17]\n"
    "ds_read_b128 v[30:33], v154 offset:608\n"
    "v_mfma_f32_16x16x32_bf16 v[18:21], v[90:93], v[114:117], v[18:21]\n"
    "v_mfma_f32_16x16x32_bf16 v[94:97], v[62:65], v[134:137], 0\n"
    "v_mfma_f32_16x16x32_bf16 v[74:77], v[62:65], v[138:141], 0\n"
    "v_mfma_f32_16x16x32_bf16 v[6:9], v[58:61], v[34:37], v[6:9]\n"
    "v_mfma_f32_16x16x32_bf16 v[10:13], v[46:49], v[34:37], v[10:13]\n"
    "ds_read_b128 v[34:37], v154 offset:1120\n"
    "v_cvt_pk_bf16_f32 v74, v94, v74\n"
    "v_mfma_f32_16x16x32_bf16 v[10:13], v[58:61], v[38:41], v[10:13]\n"
    "v_cvt_pk_bf16_f32 v75, v95, v75\n"
    "v_cvt_pk_bf16_f32 v76, v96, v76\n"
    "v_mfma_f32_16x16x32_bf16 v[14:17], v[46:49], v[38:41], v[14:17]\n"
    "ds_read_b128 v[38:41], v154 offset:1632\n"
    "s_waitcnt lgkmcnt(5)\n"
    "v_mfma_f32_16x16x32_bf16 v[14:17], v[58:61], v[42:45], v[14:17]\n"
    "v_cvt_pk_bf16_f32 v77, v97, v77\n"
    "v_mfma_f32_16x16x32_bf16 v[18:21], v[46:49], v[42:45], v[18:21]\n"
    "ds_read_b128 v[42:45], v154 offset:2144\n"
    "ds_read2_b32 v[46:47], v152 offset0:32 offset1:33\n"
    "v_mfma_f32_16x16x32_bf16 v[18:21], v[58:61], v[22:25], v[18:21]\n"
    "ds_read2_b32 v[58:59], v153 offset0:0 offset1:1\n"
    "ds_read2_b32 v[60:61], v153 offset0:2 offset1:3\n"
    "v_mfma_f32_16x16x32_bf16 v[18:21], v[74:77], v[110:113], v[18:21]\n"
    "ds_read2_b32 v[48:49], v152 offset0:34 offset1:35\n"
    "ds_read_b128 v[22:25], v154 offset:32\n"
    "v_mfma_f32_16x16x32_bf16 v[78:81], v[66:69], v[142:145], 0\n"
    "v_mfma_f32_16x16x32_bf16 v[82:85], v[70:73], v[146:149], 0\n"
    "v_mfma_f32_16x16x32_bf16 v[18:21], v[130:133], v[66:69], v[18:21]\n"
    "s_waitcnt lgkmcnt(7)\n"
    "v_mfma_f32_16x16x32_bf16 v[2:5], v[50:53], v[26:29], v[2:5]\n"
    "v_cvt_pk_bf16_f32 v82, v78, v82\n"
    "v_cvt_pk_bf16_f32 v83, v79, v83\n"
    "v_mfma_f32_16x16x32_bf16 v[6:9], v[54:57], v[26:29], v[6:9]\n"
    "ds_read_b128 v[26:29], v154 offset:544\n"
    "v_cvt_pk_bf16_f32 v84, v80, v84\n"
    "v_mfma_f32_16x16x32_bf16 v[6:9], v[50:53], v[30:33], v[6:9]\n"
    "v_cvt_pk_bf16_f32 v85, v81, v85\n"
    "v_mfma_f32_16x16x32_bf16 v[10:13], v[54:57], v[30:33], v[10:13]\n"
    "ds_read_b128 v[30:33], v154 offset:1056\n"
    "v_mfma_f32_16x16x32_bf16 v[10:13], v[50:53], v[34:37], v[10:13]\n"
    "v_mfma_f32_16x16x32_bf16 v[14:17], v[54:57], v[34:37], v[14:17]\n"
    "ds_read_b128 v[34:37], v154 offset:1568\n"
    "s_waitcnt lgkmcnt(3)\n"
    "v_mfma_f32_16x16x32_bf16 v[14:17], v[50:53], v[38:41], v[14:17]\n"
    "v_mfma_f32_16x16x32_bf16 v[18:21], v[54:57], v[38:41], v[18:21]\n"
    "ds_read_b128 v[38:41], v154 offset:2080\n"
    "v_mfma_f32_16x16x32_bf16 v[18:21], v[50:53], v[42:45], v[18:21]\n"
    "v_mfma_f32_16x16x32_bf16 v[18:21], v[82:85], v[122:125], v[18:21]\n"
    "v_mfma_f32_16x16x32_bf16 v[2:5], v[58:61], v[22:25], v[2:5]\n"
    "v_mfma_f32_16x16x32_bf16 v[6:9], v[46:49], v[22:25], v[6:9]\n"
    "s_waitcnt lgkmcnt(3)\n"
    "v_mfma_f32_16x16x32_bf16 v[6:9], v[58:61], v[26:29], v[6:9]\n"
    "v_cvt_pk_bf16_f32 v2, v2, v3\n"
    "v_cvt_pk_bf16_f32 v4, v4, v5\n"
    "v_mfma_f32_16x16x32_bf16 v[10:13], v[46:49], v[26:29], v[10:13]\n"
    "v_cmp_gt_u32 vcc, 4, v166\n"
    "s_cbranch_vccz .Lk70\n"
    "s_mov_b32 s46, 32768\n"
    ".Lk71:\n"
    "ds_read_b32 v166, v1 offset:8\n"
    "s_waitcnt lgkmcnt(0)\n"
    "v_cmp_gt_u32 vcc, 4, v166\n"
    "s_cbranch_vccz .Lk72\n"
    "s_sleep 1\n"
    "s_sub_u32 s46, s46, 1\n"
    "s_cbranch_scc0 .Lk71\n"
    ".Lk72:\n"
    ".Lk70:\n"
    "buffer_store_dword v2, v157, s[16:19], s62 offen offset:0 sc1\n"
    "s_waitcnt lgkmcnt(2)\n"
    "v_mfma_f32_16x16x32_bf16 v[10:13], v[58:61], v[30:33], v[10:13]\n"
    "buffer_store_dword v4, v157, s[16:19], s62 offen offset:256 sc1\n"
    "v_cvt_pk_bf16_f32 v6, v6, v7\n"
    "v_mfma_f32_16x16x32_bf16 v[14:17], v[46:49], v[30:33], v[14:17]\n"
    "buffer_store_dword v6, v157, s[16:19], s62 offen offset:512 sc1\n"
    "s_waitcnt lgkmcnt(1)\n"
    "v_mfma_f32_16x16x32_bf16 v[14:17], v[58:61], v[34:37], v[14:17]\n"
    "v_cvt_pk_bf16_f32 v8, v8, v9\n"
    "v_cvt_pk_bf16_f32 v10, v10, v11\n"
    "v_mfma_f32_16x16x32_bf16 v[18:21], v[46:49], v[34:37], v[18:21]\n"
    "buffer_store_dword v8, v157, s[16:19], s62 offen offset:768 sc1\n"
    "s_waitcnt lgkmcnt(0)\n"
    "v_mfma_f32_16x16x32_bf16 v[18:21], v[58:61], v[38:41], v[18:21]\n"
    "buffer_store_dword v10, v157, s[16:19], s62 offen offset:1024 sc1\n"
    "v_cvt_pk_bf16_f32 v12, v12, v13\n"
    "v_cvt_pk_bf16_f32 v14, v14, v15\n"
    "v_cvt_pk_bf16_f32 v16, v16, v17\n"
    "buffer_store_dword v12, v157, s[16:19], s62 offen offset:1280 sc1\n"
    "buffer_store_dword v14, v157, s[16:19], s62 offen offset:1536 sc1\n"
    "buffer_store_dword v16, v157, s[16:19], s62 offen offset:1792 sc1\n"
    "s_nop 0\n"
    "v_cvt_pk_bf16_f32 v18, v18, v19\n"
    "v_cvt_pk_bf16_f32 v20, v20, v21\n"
    "buffer_store_dword v18, v157, s[16:19], s62 offen offset:2048 sc1\n"
    "buffer_store_dword v20, v157, s[16:19], s62 offen offset:2304 sc1\n"
    "s_branch .Lk56\n"
    ".Lk58:\n"
    "v_lshlrev_b32 v165, 2, v157\n"
    "v_add_u32 v165, 145280, v165\n"
    "ds_read_b128 v[138:141], v165 offset:0\n"
    "ds_read_b128 v[142:145], v165 offset:1024\n"
    "ds_read_b128 v[146:149], v165 offset:2048\n"
    "ds_read_b128 v[158:161], v165 offset:3072\n"
    "ds_read2_b32 v[130:131], v152 offset0:16 offset1:17\n"
    "ds_read2_b32 v[132:133], v152 offset0:18 offset1:19\n"
    "ds_read2_b32 v[134:135], v152 offset0:0 offset1:1\n"
    "ds_read2_b32 v[136:137], v152 offset0:2 offset1:3\n"
    "ds_read2_b32 v[90:91], v151 offset0:48 offset1:49\n"
    "ds_read2_b32 v[92:93], v151 offset0:50 offset1:51\n"
    "ds_read2_b32 v[94:95], v151 offset0:32 offset1:33\n"
    "ds_read2_b32 v[96:97], v151 offset0:34 offset1:35\n"
    "ds_read2_b32 v[98:99], v151 offset0:16 offset1:17\n"
    "ds_read2_b32 v[100:101], v151 offset0:18 offset1:19\n"
    "ds_read2_b32 v[102:103], v151 offset0:96 offset1:97\n"
    "ds_read2_b32 v[104:105], v151 offset0:98 offset1:99\n"
    "ds_read2_b32 v[106:107], v151 offset0:80 offset1:81\n"
    "ds_read2_b32 v[108:109], v151 offset0:82 offset1:83\n"
    "ds_read2_b32 v[110:111], v151 offset0:64 offset1:65\n"
    "ds_read2_b32 v[112:113], v151 offset0:66 offset1:67\n"
    "ds_read2_b32 v[114:115], v151 offset0:175 offset1:176\n"
    "ds_read2_b32 v[116:117], v151 offset0:177 offset1:178\n"
    "ds_read2_b32 v[118:119], v151 offset0:159 offset1:160\n"
    "ds_read2_b32 v[120:121], v151 offset0:161 offset1:162\n"
    "ds_read2_b32 v[122:123], v151 offset0:143 offset1:144\n"
    "ds_read2_b32 v[124:125], v151 offset0:145 offset1:146\n"
    "ds_read2_b32 v[126:127], v151 offset0:127 offset1:128\n"
    "ds_read2_b32 v[128:129], v151 offset0:129 offset1:130\n"
    "s_waitcnt lgkmcnt(0)\n"
    "s_nop 0\n"
    "ds_read2_b32 v[42:43], v153 offset0:112 offset1:113\n"
    "ds_read2_b32 v[44:45], v153 offset0:114 offset1:115\n"
    "ds_read2_b32 v[46:47], v152 offset0:144 offset1:145\n"
    "ds_read2_b32 v[48:49], v152 offset0:146 offset1:147\n"
    "ds_read_b128 v[18:21], v154 offset:2528\n"
    "ds_read_b128 v[22:25], v154 offset:3040\n"
    "ds_read_b128 v[26:29], v154 offset:3552\n"
    "ds_read_b128 v[30:33], v154 offset:4064\n"
    "ds_read_b128 v[58:61], v154 offset:992\n"
    "ds_read2_b32 v[50:51], v153 offset0:96 offset1:97\n"
    "ds_read2_b32 v[52:53], v153 offset0:98 offset1:99\n"
    "ds_read2_b32 v[38:39], v152 offset0:128 offset1:129\n"
    "ds_read2_b32 v[40:41], v152 offset0:130 offset1:131\n"
    "ds_read_b128 v[34:37], v154 offset:2464\n"
    "ds_read_b128 v[62:65], v154 offset:1504\n"
    "ds_read_b128 v[14:17], v154 offset:2976\n"
    "s_waitcnt lgkmcnt(11)\n"
    "v_mfma_f32_16x16x32_bf16 v[2:5], v[46:49], v[18:21], 0\n"
    "ds_read_b128 v[18:21], v154 offset:3488\n"
    "s_waitcnt lgkmcnt(8)\n"
    "v_mfma_f32_16x16x32_bf16 v[2:5], v[42:45], v[22:25], v[2:5]\n"
    "ds_read_b128 v[54:57], v154 offset:2016\n"
    "v_mfma_f32_16x16x32_bf16 v[6:9], v[46:49], v[22:25], 0\n"
    "ds_read_b128 v[22:25], v154 offset:4000\n"
    "v_mfma_f32_16x16x32_bf16 v[6:9], v[42:45], v[26:29], v[6:9]\n"
    "v_mfma_f32_16x16x32_bf16 v[10:13], v[46:49], v[26:29], 0\n"
    "ds_read2_b32 v[46:47], v152 offset0:112 offset1:113\n"
    "ds_read2_b32 v[48:49], v152 offset0:114 offset1:115\n"
    "v_mfma_f32_16x16x32_bf16 v[10:13], v[42:45], v[30:33], v[10:13]\n"
    "ds_read2_b32 v[42:43], v153 offset0:80 offset1:81\n"
    "ds_read2_b32 v[44:45], v153 offset0:82 offset1:83\n"
    "v_mfma_f32_16x16x32_bf16 v[70:73], v[58:61], v[138:141], 0\n"
    "ds_read_b128 v[26:29], v154 offset:2400\n"
    "v_mfma_f32_16x16x32_bf16 v[74:77], v[58:61], v[142:145], 0\n"
    "ds_read_b128 v[58:61], v154 offset:2528\n"
    "s_waitcnt lgkmcnt(10)\n"
    "v_mfma_f32_16x16x32_bf16 v[2:5], v[38:41], v[34:37], v[2:5]\n"
    "ds_read_b128 v[30:33], v154 offset:2912\n"
    "ds_read_b128 v[34:37], v154 offset:3424\n"
    "v_mfma_f32_16x16x32_bf16 v[78:81], v[62:65], v[138:141], 0\n"
    "v_cvt_pk_bf16_f32 v74, v70, v74\n"
    "v_cvt_pk_bf16_f32 v75, v71, v75\n"
    "v_mfma_f32_16x16x32_bf16 v[82:85], v[62:65], v[142:145], 0\n"
    "v_cvt_pk_bf16_f32 v76, v72, v76\n"
    "s_waitcnt lgkmcnt(9)\n"
    "v_mfma_f32_16x16x32_bf16 v[2:5], v[50:53], v[14:17], v[2:5]\n"
    "ds_read_b128 v[62:65], v154 offset:3040\n"
    "v_cvt_pk_bf16_f32 v77, v73, v77\n"
    "v_mfma_f32_16x16x32_bf16 v[6:9], v[38:41], v[14:17], v[6:9]\n"
    "v_cvt_pk_bf16_f32 v82, v78, v82\n"
    "v_cvt_pk_bf16_f32 v83, v79, v83\n"
    "v_mfma_f32_16x16x32_bf16 v[2:5], v[74:77], v[98:101], v[2:5]\n"
    "v_cvt_pk_bf16_f32 v84, v80, v84\n"
    "v_cvt_pk_bf16_f32 v85, v81, v85\n"
    "v_mfma_f32_16x16x32_bf16 v[6:9], v[50:53], v[18:21], v[6:9]\n"
    "ds_read_b128 v[14:17], v154 offset:3936\n"
    "v_mfma_f32_16x16x32_bf16 v[10:13], v[38:41], v[18:21], v[10:13]\n"
    "ds_read2_b32 v[38:39], v152 offset0:96 offset1:97\n"
    "ds_read2_b32 v[40:41], v152 offset0:98 offset1:99\n"
    "v_mfma_f32_16x16x32_bf16 v[2:5], v[82:85], v[94:97], v[2:5]\n"
    "ds_read_b128 v[18:21], v154 offset:2336\n"
    "v_mfma_f32_16x16x32_bf16 v[6:9], v[82:85], v[98:101], v[6:9]\n"
    "v_mfma_f32_16x16x32_bf16 v[86:89], v[54:57], v[138:141], 0\n"
    "v_mfma_f32_16x16x32_bf16 v[66:69], v[54:57], v[142:145], 0\n"
    "v_mfma_f32_16x16x32_bf16 v[2:5], v[130:133], v[54:57], v[2:5]\n"
    "ds_read_b128 v[54:57], v154 offset:976\n"
    "s_waitcnt lgkmcnt(5)\n"
    "v_mfma_f32_16x16x32_bf16 v[10:13], v[50:53], v[22:25], v[10:13]\n"
    "v_cvt_pk_bf16_f32 v66, v86, v66\n"
    "v_cvt_pk_bf16_f32 v67, v87, v67\n"
    "v_mfma_f32_16x16x32_bf16 v[2:5], v[46:49], v[26:29], v[2:5]\n"
    "v_cvt_pk_bf16_f32 v68, v88, v68\n"
    "v_cvt_pk_bf16_f32 v69, v89, v69\n"
    "v_mfma_f32_16x16x32_bf16 v[70:73], v[58:61], v[138:141], 0\n"
    "ds_read2_b32 v[50:51], v153 offset0:64 offset1:65\n"
    "ds_read2_b32 v[52:53], v153 offset0:66 offset1:67\n"
    "v_mfma_f32_16x16x32_bf16 v[2:5], v[66:69], v[90:93], v[2:5]\n"
    "ds_read_b128 v[22:25], v154 offset:2848\n"
    "ds_read_b128 v[26:29], v154 offset:3360\n"
    "v_mfma_f32_16x16x32_bf16 v[6:9], v[66:69], v[94:97], v[6:9]\n"
    "v_mfma_f32_16x16x32_bf16 v[10:13], v[66:69], v[98:101], v[10:13]\n"
    "v_mfma_f32_16x16x32_bf16 v[74:77], v[58:61], v[142:145], 0\n"
    "v_mfma_f32_16x16x32_bf16 v[6:9], v[130:133], v[58:61], v[6:9]\n"
    "ds_read_b128 v[58:61], v154 offset:1488\n"
    "v_mfma_f32_16x16x32_bf16 v[2:5], v[42:45], v[30:33], v[2:5]\n"
    "v_cvt_pk_bf16_f32 v74, v70, v74\n"
    "v_cvt_pk_bf16_f32 v75, v71, v75\n"
    "v_mfma_f32_16x16x32_bf16 v[6:9], v[46:49], v[30:33], v[6:9]\n"
    "v_cvt_pk_bf16_f32 v76, v72, v76\n"
    "v_cvt_pk_bf16_f32 v77, v73, v77\n"
    "v_mfma_f32_16x16x32_bf16 v[78:81], v[62:65], v[138:141], 0\n"
    "ds_read_b128 v[30:33], v154 offset:3872\n"
    "v_mfma_f32_16x16x32_bf16 v[6:9], v[74:77], v[90:93], v[6:9]\n"
    "v_mfma_f32_16x16x32_bf16 v[10:13], v[74:77], v[94:97], v[10:13]\n"
    "v_mfma_f32_16x16x32_bf16 v[82:85], v[62:65], v[142:145], 0\n"
    "v_mfma_f32_16x16x32_bf16 v[10:13], v[130:133], v[62:65], v[10:13]\n"
    "ds_read_b128 v[62:65], v154 offset:2000\n"
    "v_mfma_f32_16x16x32_bf16 v[6:9], v[42:45], v[34:37], v[6:9]\n"
    "v_cvt_pk_bf16_f32 v82, v78, v82\n"
    "v_cvt_pk_bf16_f32 v83, v79, v83\n"
    "v_mfma_f32_16x16x32_bf16 v[10:13], v[46:49], v[34:37], v[10:13]\n"
    "v_cvt_pk_bf16_f32 v84, v80, v84\n"
    "s_waitcnt lgkmcnt(2)\n"
    "v_mfma_f32_16x16x32_bf16 v[10:13], v[42:45], v[14:17], v[10:13]\n"
    "ds_read2_b32 v[42:43], v153 offset0:48 offset1:49\n"
    "v_cvt_pk_bf16_f32 v85, v81, v85\n"
    "v_mfma_f32_16x16x32_bf16 v[86:89], v[54:57], v[138:141], 0\n"
    "ds_read2_b32 v[44:45], v153 offset0:50 offset1:51\n"
    "ds_read2_b32 v[46:47], v152 offset0:80 offset1:81\n"
    "v_mfma_f32_16x16x32_bf16 v[10:13], v[82:85], v[90:93], v[10:13]\n"
    "ds_read2_b32 v[48:49], v152 offset0:82 offset1:83\n"
    "ds_read_b128 v[34:37], v154 offset:2272\n"
    "v_mfma_f32_16x16x32_bf16 v[66:69], v[54:57], v[142:145], 0\n"
    "ds_read_b128 v[54:57], v154 offset:2512\n"
    "ds_read_b128 v[14:17], v154 offset:2784\n"
    "v_mfma_f32_16x16x32_bf16 v[2:5], v[38:41], v[18:21], v[2:5]\n"
    "ds_read_b128 v[18:21], v154 offset:3296\n"
    "v_mfma_f32_16x16x32_bf16 v[2:5], v[50:53], v[22:25], v[2:5]\n"
    "v_cvt_pk_bf16_f32 v66, v86, v66\n"
    "v_cvt_pk_bf16_f32 v67, v87, v67\n"
    "v_mfma_f32_16x16x32_bf16 v[6:9], v[38:41], v[22:25], v[6:9]\n"
    "v_cvt_pk_bf16_f32 v68, v88, v68\n"
    "v_cvt_pk_bf16_f32 v69, v89, v69\n"
    "v_mfma_f32_16x16x32_bf16 v[70:73], v[58:61], v[138:141], 0\n"
    "ds_read_b128 v[22:25], v154 offset:3808\n"
    "v_mfma_f32_16x16x32_bf16 v[2:5], v[66:69], v[110:113], v[2:5]\n"
    "v_mfma_f32_16x16x32_bf16 v[74:77], v[58:61], v[142:145], 0\n"
    "ds_read_b128 v[58:61], v154 offset:3024\n"
    "v_mfma_f32_16x16x32_bf16 v[6:9], v[50:53], v[26:29], v[6:9]\n"
    "v_mfma_f32_16x16x32_bf16 v[10:13], v[38:41], v[26:29], v[10:13]\n"
    "v_cvt_pk_bf16_f32 v74, v70, v74\n"
    "s_waitcnt lgkmcnt(5)\n"
    "v_mfma_f32_16x16x32_bf16 v[10:13], v[50:53], v[30:33], v[10:13]\n"
    "v_cvt_pk_bf16_f32 v75, v71, v75\n"
    "v_cvt_pk_bf16_f32 v76, v72, v76\n"
    "v_mfma_f32_16x16x32_bf16 v[78:81], v[62:65], v[138:141], 0\n"
    "ds_read2_b32 v[50:51], v153 offset0:32 offset1:33\n"
    "v_cvt_pk_bf16_f32 v77, v73, v77\n"
    "v_mfma_f32_16x16x32_bf16 v[82:85], v[62:65], v[142:145], 0\n"
    "ds_read2_b32 v[52:53], v153 offset0:34 offset1:35\n"
    "ds_read2_b32 v[38:39], v152 offset0:64 offset1:65\n"
    "v_mfma_f32_16x16x32_bf16 v[2:5], v[74:77], v[106:109], v[2:5]\n"
    "ds_read2_b32 v[40:41], v152 offset0:66 offset1:67\n"
    "ds_read_b128 v[26:29], v154 offset:2208\n"
    "v_mfma_f32_16x16x32_bf16 v[6:9], v[74:77], v[110:113], v[6:9]\n"
    "v_cvt_pk_bf16_f32 v82, v78, v82\n"
    "v_cvt_pk_bf16_f32 v83, v79, v83\n"
    "v_mfma_f32_16x16x32_bf16 v[2:5], v[46:49], v[34:37], v[2:5]\n"
    "v_cvt_pk_bf16_f32 v84, v80, v84\n"
    "s_waitcnt lgkmcnt(6)\n"
    "v_mfma_f32_16x16x32_bf16 v[86:89], v[54:57], v[138:141], 0\n"
    "ds_read_b128 v[62:65], v154 offset:480\n"
    "v_cvt_pk_bf16_f32 v85, v81, v85\n"
    "v_mfma_f32_16x16x32_bf16 v[66:69], v[54:57], v[142:145], 0\n"
    "ds_read_b128 v[54:57], v154 offset:464\n"
    "ds_read_b128 v[30:33], v154 offset:2720\n"
    "v_mfma_f32_16x16x32_bf16 v[2:5], v[82:85], v[102:105], v[2:5]\n"
    "ds_read_b128 v[34:37], v154 offset:3232\n"
    "v_mfma_f32_16x16x32_bf16 v[6:9], v[82:85], v[106:109], v[6:9]\n"
    "v_cvt_pk_bf16_f32 v66, v86, v66\n"
    "v_cvt_pk_bf16_f32 v67, v87, v67\n"
    "v_mfma_f32_16x16x32_bf16 v[10:13], v[82:85], v[110:113], v[10:13]\n"
    "v_cvt_pk_bf16_f32 v68, v88, v68\n"
    "v_cvt_pk_bf16_f32 v69, v89, v69\n"
    "v_mfma_f32_16x16x32_bf16 v[2:5], v[42:45], v[14:17], v[2:5]\n"
    "s_waitcnt lgkmcnt(6)\n"
    "ds_read_b32 v166, v1 offset:8\n"
    "v_mfma_f32_16x16x32_bf16 v[6:9], v[46:49], v[14:17], v[6:9]\n"
    "ds_read_b128 v[14:17], v154 offset:3744\n"
    "v_mfma_f32_16x16x32_bf16 v[6:9], v[42:45], v[18:21], v[6:9]\n"
    "v_mfma_f32_16x16x32_bf16 v[10:13], v[46:49], v[18:21], v[10:13]\n"
    "ds_read2_b32 v[46:47], v152 offset0:48 offset1:49\n"
    "ds_read2_b32 v[48:49], v152 offset0:50 offset1:51\n"
    "v_mfma_f32_16x16x32_bf16 v[6:9], v[66:69], v[102:105], v[6:9]\n"
    "ds_read_b128 v[18:21], v154 offset:2144\n"
    "v_mfma_f32_16x16x32_bf16 v[10:13], v[66:69], v[106:109], v[10:13]\n"
    "v_mfma_f32_16x16x32_bf16 v[70:73], v[58:61], v[138:141], 0\n"
    "v_mfma_f32_16x16x32_bf16 v[74:77], v[58:61], v[142:145], 0\n"
    "ds_read_b128 v[58:61], v154 offset:992\n"
    "v_mfma_f32_16x16x32_bf16 v[10:13], v[42:45], v[22:25], v[10:13]\n"
    "ds_read2_b32 v[42:43], v153 offset0:16 offset1:17\n"
    "s_waitcnt lgkmcnt(7)\n"
    "v_mfma_f32_16x16x32_bf16 v[2:5], v[38:41], v[26:29], v[2:5]\n"
    "v_cvt_pk_bf16_f32 v74, v70, v74\n"
    "v_cvt_pk_bf16_f32 v75, v71, v75\n"
    "v_mfma_f32_16x16x32_bf16 v[78:81], v[62:65], v[146:149], 0\n"
    "ds_read_b128 v[62:65], v154 offset:976\n"
    "v_cvt_pk_bf16_f32 v76, v72, v76\n"
    "v_mfma_f32_16x16x32_bf16 v[82:85], v[54:57], v[158:161], 0\n"
    "ds_read_b128 v[54:57], v154 offset:1504\n"
    "v_cvt_pk_bf16_f32 v77, v73, v77\n"
    "v_mfma_f32_16x16x32_bf16 v[2:5], v[50:53], v[30:33], v[2:5]\n"
    "ds_read2_b32 v[44:45], v153 offset0:18 offset1:19\n"
    "ds_read_b128 v[22:25], v154 offset:2656\n"
    "v_mfma_f32_16x16x32_bf16 v[10:13], v[74:77], v[102:105], v[10:13]\n"
    "v_cvt_pk_bf16_f32 v82, v78, v82\n"
    "v_cvt_pk_bf16_f32 v83, v79, v83\n"
    "v_mfma_f32_16x16x32_bf16 v[6:9], v[38:41], v[30:33], v[6:9]\n"
    "v_cvt_pk_bf16_f32 v84, v80, v84\n"
    "v_cvt_pk_bf16_f32 v85, v81, v85\n"
    "v_mfma_f32_16x16x32_bf16 v[6:9], v[50:53], v[34:37], v[6:9]\n"
    "ds_read_b128 v[26:29], v154 offset:3168\n"
    "ds_read_b128 v[30:33], v154 offset:3680\n"
    "v_mfma_f32_16x16x32_bf16 v[2:5], v[82:85], v[118:121], v[2:5]\n"
    "v_mfma_f32_16x16x32_bf16 v[6:9], v[82:85], v[122:125], v[6:9]\n"
    "v_mfma_f32_16x16x32_bf16 v[10:13], v[82:85], v[126:129], v[10:13]\n"
    "s_waitcnt lgkmcnt(6)\n"
    "v_mfma_f32_16x16x32_bf16 v[86:89], v[58:61], v[146:149], 0\n"
    "s_waitcnt lgkmcnt(5)\n"
    "v_mfma_f32_16x16x32_bf16 v[66:69], v[62:65], v[158:161], 0\n"
    "ds_read_b128 v[62:65], v154 offset:2016\n"
    "v_mfma_f32_16x16x32_bf16 v[2:5], v[134:137], v[58:61], v[2:5]\n"
    "ds_read_b128 v[58:61], v154 offset:1488\n"
    "v_mfma_f32_16x16x32_bf16 v[10:13], v[38:41], v[34:37], v[10:13]\n"
    "v_cvt_pk_bf16_f32 v66, v86, v66\n"
    "v_cvt_pk_bf16_f32 v67, v87, v67\n"
    "v_mfma_f32_16x16x32_bf16 v[10:13], v[50:53], v[14:17], v[10:13]\n"
    "v_cvt_pk_bf16_f32 v68, v88, v68\n"
    "s_waitcnt lgkmcnt(4)\n"
    "v_mfma_f32_16x16x32_bf16 v[70:73], v[54:57], v[146:149], 0\n"
    "ds_read2_b32 v[50:51], v153 offset0:0 offset1:1\n"
    "v_cvt_pk_bf16_f32 v69, v89, v69\n"
    "v_mfma_f32_16x16x32_bf16 v[6:9], v[134:137], v[54:57], v[6:9]\n"
    "ds_read_b128 v[54:57], v154 offset:2000\n"
    "ds_read2_b32 v[52:53], v153 offset0:2 offset1:3\n"
    "v_mfma_f32_16x16x32_bf16 v[2:5], v[66:69], v[114:117], v[2:5]\n"
    "ds_read2_b32 v[38:39], v152 offset0:32 offset1:33\n"
    "ds_read2_b32 v[40:41], v152 offset0:34 offset1:35\n"
    "v_mfma_f32_16x16x32_bf16 v[6:9], v[66:69], v[118:121], v[6:9]\n"
    "ds_read_b128 v[34:37], v154 offset:2080\n"
    "ds_read_b128 v[14:17], v154 offset:2592\n"
    "v_mfma_f32_16x16x32_bf16 v[10:13], v[66:69], v[122:125], v[10:13]\n"
    "v_mfma_f32_16x16x32_bf16 v[2:5], v[46:49], v[18:21], v[2:5]\n"
    "ds_read_b128 v[18:21], v154 offset:3104\n"
    "s_waitcnt lgkmcnt(8)\n"
    "v_mfma_f32_16x16x32_bf16 v[74:77], v[58:61], v[158:161], 0\n"
    "v_mfma_f32_16x16x32_bf16 v[2:5], v[42:45], v[22:25], v[2:5]\n"
    "v_mfma_f32_16x16x32_bf16 v[6:9], v[46:49], v[22:25], v[6:9]\n"
    "v_cvt_pk_bf16_f32 v74, v70, v74\n"
    "v_cvt_pk_bf16_f32 v75, v71, v75\n"
    "v_mfma_f32_16x16x32_bf16 v[78:81], v[62:65], v[146:149], 0\n"
    "v_cvt_pk_bf16_f32 v76, v72, v76\n"
    "s_waitcnt lgkmcnt(6)\n"
    "v_mfma_f32_16x16x32_bf16 v[82:85], v[54:57], v[158:161], 0\n"
    "ds_read_b128 v[22:25], v154 offset:3616\n"
    "v_cvt_pk_bf16_f32 v77, v73, v77\n"
    "v_mfma_f32_16x16x32_bf16 v[10:13], v[134:137], v[62:65], v[10:13]\n"
    "v_mfma_f32_16x16x32_bf16 v[6:9], v[42:45], v[26:29], v[6:9]\n"
    "v_cvt_pk_bf16_f32 v82, v78, v82\n"
    "v_cvt_pk_bf16_f32 v83, v79, v83\n"
    "v_mfma_f32_16x16x32_bf16 v[6:9], v[74:77], v[114:117], v[6:9]\n"
    "v_cvt_pk_bf16_f32 v84, v80, v84\n"
    "v_cvt_pk_bf16_f32 v85, v81, v85\n"
    "v_mfma_f32_16x16x32_bf16 v[10:13], v[74:77], v[118:121], v[10:13]\n"
    "v_mfma_f32_16x16x32_bf16 v[10:13], v[46:49], v[26:29], v[10:13]\n"
    "v_mfma_f32_16x16x32_bf16 v[10:13], v[42:45], v[30:33], v[10:13]\n"
    "v_mfma_f32_16x16x32_bf16 v[10:13], v[82:85], v[114:117], v[10:13]\n"
    "s_waitcnt lgkmcnt(1)\n"
    "v_mfma_f32_16x16x32_bf16 v[2:5], v[38:41], v[34:37], v[2:5]\n"
    "v_mfma_f32_16x16x32_bf16 v[2:5], v[50:53], v[14:17], v[2:5]\n"
    "v_mfma_f32_16x16x32_bf16 v[6:9], v[38:41], v[14:17], v[6:9]\n"
    "v_mfma_f32_16x16x32_bf16 v[6:9], v[50:53], v[18:21], v[6:9]\n"
    "v_cvt_pk_bf16_f32 v2, v2, v3\n"
    "v_cvt_pk_bf16_f32 v4, v4, v5\n"
    "v_mfma_f32_16x16x32_bf16 v[10:13], v[38:41], v[18:21], v[10:13]\n"
    "v_cmp_gt_u32 vcc, 4, v166\n"
    "s_cbranch_vccz .Lk73\n"
    "s_mov_b32 s46, 32768\n"
    ".Lk74:\n"
    "ds_read_b32 v166, v1 offset:8\n"
    "s_waitcnt lgkmcnt(0)\n"
    "v_cmp_gt_u32 vcc, 4, v166\n"
    "s_cbranch_vccz .Lk75\n"
    "s_sleep 1\n"
    "s_sub_u32 s46, s46, 1\n"
    "s_cbranch_scc0 .Lk74\n"
    ".Lk75:\n"
    ".Lk73:\n"
    "buffer_store_dword v2, v157, s[16:19], s62 offen offset:2560 sc1\n"
    "s_waitcnt lgkmcnt(0)\n"
    "v_mfma_f32_16x16x32_bf16 v[10:13], v[50:53], v[22:25], v[10:13]\n"
    "buffer_store_dword v4, v157, s[16:19], s62 offen offset:2816 sc1\n"
    "v_cvt_pk_bf16_f32 v6, v6, v7\n"
    "v_cvt_pk_bf16_f32 v8, v8, v9\n"
    "buffer_store_dword v6, v157, s[16:19], s62 offen offset:3072 sc1\n"
    "buffer_store_dword v8, v157, s[16:19], s62 offen offset:3328 sc1\n"
    "s_nop 2\n"
    "v_cvt_pk_bf16_f32 v10, v10, v11\n"
    "v_cvt_pk_bf16_f32 v12, v12, v13\n"
    "buffer_store_dword v10, v157, s[16:19], s62 offen offset:3584 sc1\n"
    "buffer_store_dword v12, v157, s[16:19], s62 offen offset:3840 sc1\n"
    "s_branch .Lk56\n"
    ".Lk50:\n"
    "v_mov_b32 v2, -1\n"
    "v_mov_b32 v3, -1\n"
    "v_mov_b32 v4, -1\n"
    "v_mov_b32 v5, -1\n"
    "v_lshlrev_b32 v167, 4, v0\n"
    "s_sub_u32 s53, s50, s44\n"
    "s_add_u32 s53, s53, 5\n"
    "s_mul_i32 s53, s53, 4224\n"
    "s_add_u32 s53, s53, 3145728\n"
    "buffer_store_dwordx4 v[2:5], v167, s[16:19], s53 offen sc1\n"
    "s_sub_u32 s53, s53, 4224\n"
    "buffer_store_dwordx4 v[2:5], v167, s[16:19], s53 offen sc1\n"
    "s_sub_u32 s53, s53, 4224\n"
    "buffer_store_dwordx4 v[2:5], v167, s[16:19], s53 offen sc1\n"
    "s_sub_u32 s53, s53, 4224\n"
    "buffer_store_dwordx4 v[2:5], v167, s[16:19], s53 offen sc1\n"
    "s_sub_u32 s53, s53, 4224\n"
    "s_cmp_eq_u32 s52, 10\n"
    "s_cselect_b32 s53, 2.0, s53\n"
    "buffer_store_dwordx4 v[2:5], v167, s[16:19], s53 offen sc1\n"
    "s_sub_u32 s53, s53, 4224\n"
    "buffer_store_dwordx4 v[2:5], v167, s[16:19], s53 offen sc1\n"
    "s_waitcnt vmcnt(0)\n"
    "s_lshl_b32 s59, s51, 8\n"
    "s_add_u32 s59, s59, 1536\n"
    "s_lshl_b32 s53, s52, 2\n"
    "s_add_u32 s53, s53, s44\n"
    "s_lshl_b32 s53, s53, 2\n"
    "s_add_u32 s53, s53, s59\n"
    "v_mov_b32 v165, s56\n"
    "v_mov_b32 v167, 1\n"
    "s_mov_b64 exec, 1\n"
    "ds_add_u32 v1, v167 offset:8\n"
    "buffer_store_dword v165, off, s[16:19], s53 sc1\n"
    "s_mov_b64 exec, -1\n"
    "s_cmp_eq_u32 s44, 0\n"
    "s_cbranch_scc0 .Lk76\n"
    "v_min_u32 v165, 172, v157\n"
    "s_sleep 36\n"
    "s_mov_b32 s46, 32768\n"
    ".Lk77:\n"
    "buffer_load_dword v167, v165, s[16:19], s59 offen sc1\n"
    "s_waitcnt vmcnt(0)\n"
    "v_cmp_ne_u32 vcc, s56, v167\n"
    "s_cbranch_vccz .Lk78\n"
    "s_sleep 8\n"
    "s_sub_u32 s46, s46, 1\n"
    "s_cbranch_scc0 .Lk77\n"
    ".Lk78:\n"
    ".Lk76:\n"
    ".Lk56:\n"
    "s_cmp_ge_u32 s44, 8\n"
    "s_cbranch_scc0 .Lk79\n"
    "s_barrier\n"
    ".Lk79:\n"
    "s_mul_i32 s65, s51, 270336\n"
    "s_mul_i32 s53, s52, 384\n"
    "s_add_u32 s65, s65, s53\n"
    "v_lshrrev_b32 v160, 3, v0\n"
    "s_add_u32 s65, s65, 3145728\n"
    "v_mul_u32_u24 v159, 49, v160\n"
    "s_lshr_b32 s54, s44, 2\n"
    "s_mul_i32 s61, s52, 3\n"
    "v_lshrrev_b32 v159, 10, v159\n"
    "s_add_u32 s61, s61, s54\n"
    "v_mul_u32_u24 v158, 21, v159\n"
    "s_add_u32 s59, s65, 4224\n"
    "v_sub_u32 v158, v160, v158\n"
    "v_cmp_gt_u32 vcc, 63, v160\n"
    "v_subrev_u32 v160, 63, v160\n"
    "s_cmp_eq_u32 s52, 10\n"
    "v_mul_u32_u24 v157, 47, v160\n"
    "s_cselect_b32 s60, 1, 2\n"
    "v_lshrrev_b32 v157, 9, v157\n"
    "s_lshr_b32 s53, s61, 2\n"
    "s_mul_i32 s62, s54, 9216\n"
    "v_mul_u32_u24 v156, 11, v157\n"
    "s_lshl_b32 s53, s53, 8\n"
    "v_sub_u32 v156, v160, v156\n"
    "s_and_b32 s54, s61, 1\n"
    "v_lshrrev_b32 v163, 1, v158\n"
    "v_mad_u32_u24 v156, v156, 3, 2\n"
    "s_lshl_b32 s54, s54, 3\n"
    "v_bfe_u32 v1, v0, 3, 5\n"
    "v_add_u32 v158, v158, v163\n"
    "v_min_u32 v156, 31, v156\n"
    "s_add_u32 s53, s53, s54\n"
    "v_cndmask_b32 v158, v156, v158, vcc\n"
    "v_and_b32 v166, 3, v1\n"
    "s_and_b32 s54, s61, 2\n"
    "s_waitcnt lgkmcnt(14)\n"
    "v_and_b32 v167, 7, v0\n"
    "v_cndmask_b32 v159, v157, v159, vcc\n"
    "v_mul_u32_u24 v161, 8448, v158\n"
    "v_lshlrev_b32 v166, 6, v166\n"
    "v_and_b32 v165, 20, v1\n"
    "v_bfe_u32 v162, v1, 3, 1\n"
    "s_add_u32 s53, s53, s54\n"
    "v_mul_u32_u24 v163, 144, v167\n"
    "v_mul_u32_u24 v157, 9216, v159\n"
    "v_lshl_add_u32 v161, v167, 4, v161\n"
    "v_add3_u32 v166, v166, v165, v162\n"
    "s_mul_i32 s53, s53, 2304\n"
    "s_lshl_b32 s54, s51, 7\n"
    "v_lshl_add_u32 v163, v158, 2, v163\n"
    "v_min_u32 v159, s60, v159\n"
    "v_lshlrev_b32 v167, 4, v167\n"
    "v_mul_u32_u24 v164, 144, v1\n"
    "v_mul_u32_u24 v166, 2304, v166\n"
    "s_add_u32 s53, s53, s54\n"
    "v_add_u32 v163, v163, v157\n"
    "v_lshl_add_u32 v161, v159, 7, v161\n"
    "v_add3_u32 v164, v164, v167, s62\n"
    "v_add_u32 v166, v166, v167\n"
    "s_add_u32 s54, s53, 73728\n"
    "s_cmp_ge_u32 s44, 8\n"
    "s_cbranch_scc1 .Lk80\n"
    "s_barrier\n"
    "s_cmp_eq_u32 s44, 7\n"
    "s_cbranch_scc0 .Lk81\n"
    "s_sleep 4\n"
    ".Lk81:\n"
    ".Lk80:\n"
    "s_cmp_ge_u32 s44, 7\n"
    "s_cbranch_scc0 .Lk82\n"
    "s_sleep 8\n"
    ".Lk82:\n"
    "s_mov_b32 s46, 32768\n"
    ".Lk83:\n"
    "buffer_load_dwordx4 v[2:5], v161, s[16:19], s65 offen sc1\n"
    "buffer_load_dwordx4 v[6:9], v161, s[16:19], s59 offen sc1\n"
    "s_waitcnt vmcnt(1)\n"
    "v_max3_u32 v165, v2, v3, v4\n"
    "s_waitcnt vmcnt(0)\n"
    "v_max3_u32 v165, v165, v5, v6\n"
    "v_max3_u32 v165, v165, v7, v8\n"
    "v_max_u32 v165, v165, v9\n"
    "v_cmp_eq_u32 vcc, -1, v165\n"
    "s_cbranch_vccz .Lk84\n"
    "s_sub_u32 s46, s46, 1\n"
    "s_cbranch_scc0 .Lk83\n"
    ".Lk84:\n"
    "v_perm_b32 v10, v6, v2, s63\n"
    "v_perm_b32 v11, v6, v2, s64\n"
    "v_perm_b32 v12, v7, v3, s63\n"
    "v_perm_b32 v13, v7, v3, s64\n"
    "v_perm_b32 v14, v8, v4, s63\n"
    "v_perm_b32 v15, v8, v4, s64\n"
    "v_perm_b32 v16, v9, v5, s63\n"
    "v_perm_b32 v17, v9, v5, s64\n"
    "ds_write_b32 v163, v10 offset:0\n"
    "ds_write_b32 v163, v11 offset:1152\n"
    "ds_write_b32 v163, v12 offset:2304\n"
    "ds_write_b32 v163, v13 offset:3456\n"
    "ds_write_b32 v163, v14 offset:4608\n"
    "ds_write_b32 v163, v15 offset:5760\n"
    "ds_write_b32 v163, v16 offset:6912\n"
    "ds_write_b32 v163, v17 offset:8064\n"
    "s_waitcnt lgkmcnt(0)\n"
    "s_barrier\n"
    "s_cmp_ge_u32 s61, 32\n"
    "s_cbranch_scc0 .Lk85\n"
    "s_endpgm\n"
    ".Lk85:\n"
    "ds_read_b128 v[2:5], v164\n"
    "ds_read_b128 v[6:9], v164 offset:4608\n"
    "s_waitcnt lgkmcnt(1)\n"
    "buffer_store_dwordx4 v[2:5], v166, s[20:23], s53 offen sc1\n"
    "s_waitcnt lgkmcnt(0)\n"
    "buffer_store_dwordx4 v[6:9], v166, s[20:23], s54 offen sc1\n"
    "s_or_b32 s53, s52, s44\n"
    "s_cbranch_scc1 .Lk86\n"
    "s_lshl_b32 s54, s51, 7\n"
    "s_mov_b64 exec, 1\n"
    "v_mov_b32 v165, s56\n"
    "buffer_store_dword v165, off, s[16:19], s54 offset:128 sc1\n"
    ".Lk86:\n"
    "s_endpgm\n"
    ".LkBrest:\n"
    "s_lshl_b32 s50, s49, 4\n"
    "s_add_u32 s55, s50, s48\n"
    "s_sub_u32 s56, s50, s48\n"
    "s_sub_u32 s56, s56, 97\n"
    "s_cmp_lt_u32 s49, 7\n"
    "s_cselect_b32 s50, s55, s56\n"
    "s_lshl_b32 s55, s49, 3\n"
    "s_add_u32 s55, s55, s48\n"
    "s_cmp_lt_u32 s49, 14\n"
    "s_cselect_b32 s50, s50, s55\n"
    "s_cmp_lt_u32 s49, 16\n"
    "s_cselect_b32 s50, s50, -1\n"
    "s_cmp_eq_u32 s45, 135\n"
    "s_cselect_b32 s50, 128, s50\n"
    "s_and_b32 s55, s50, 15\n"
    "s_lshl_b32 s55, s55, 4\n"
    "s_lshr_b32 s56, s50, 4\n"
    "s_add_u32 s51, s55, s56\n"
    "s_add_u32 s51, s51, 1\n"
    "s_sub_i32 s55, s50, 120\n"
    "s_lshl_b32 s55, s55, 4\n"
    "s_cmp_ge_i32 s50, 120\n"
    "s_cselect_b32 s51, s55, s51\n"
    "s_lshl_b32 s55, s44, 11\n"
    "v_bfe_u32 v162, v0, 3, 2\n"
    "v_and_b32 v163, 2, v0\n"
    "s_cmp_eq_u32 s44, 0\n"
    "v_lshl_or_b32 v162, v162, 2, v163\n"
    "v_bfe_u32 v158, v0, 2, 1\n"
    "s_cselect_b32 s55, 17408, s55\n"
    "v_or_b32 v162, v162, v158\n"
    "v_bfe_i32 v153, v0, 0, 1\n"
    "s_add_u32 s52, s52, s55\n"
    "v_cvt_f32_u32 v158, v162\n"
    "v_and_b32 v161, 0x3e800000, v153\n"
    "v_cvt_f32_u32 v156, v166\n"
    "s_add_u32 s52, s52, 63488\n"
    "v_and_b32 v153, 2147516416, v153\n"
    "v_cvt_f32_u32 v160, v165\n"
    "v_cvt_f32_u32 v159, s44\n"
    "v_mul_f32 v158, 0x3d800000, v158\n"
    "v_mul_f32 v156, 4.0, v156\n"
    "v_lshlrev_b32 v155, 4, v1\n"
    "s_cmp_ge_u32 s44, 9\n"
    "s_cbranch_scc1 .Lk7\n"
    "v_mul_f32_e64 v143, 4.0, v160\n"
    "v_bfe_i32 v152, v0, 2, 1\n"
    "v_lshl_add_u32 v149, v165, 4, v167\n"
    "v_and_b32 v152, 2147516416, v152\n"
    "v_mul_u32_u24 v149, 80, v149\n"
    "s_cmp_ge_u32 s44, 8\n"
    "s_cbranch_scc1 .Lk10\n"
    "v_fma_f32 v146, v143, v158, v161\n"
    "v_add_f32 v140, v158, v146\n"
    "v_fma_f32 v137, v158, 2.0, v146\n"
    "v_fmamk_f32 v134, v158, 0x40400000, v146\n"
    "v_cos_f32 v142, v146\n"
    "v_sin_f32 v141, v146\n"
    "v_cos_f32 v139, v140\n"
    "v_sin_f32 v138, v140\n"
    "v_cos_f32 v136, v137\n"
    "v_sin_f32 v135, v137\n"
    "v_cos_f32 v133, v134\n"
    "v_sin_f32 v132, v134\n"
    "v_cmp_le_u32 vcc, 9, v162\n"
    "v_cvt_pk_bf16_f32 v26, v142, v141\n"
    "v_cvt_pk_bf16_f32 v27, v139, v138\n"
    "v_cvt_pk_bf16_f32 v28, v136, v135\n"
    "v_cvt_pk_bf16_f32 v29, v133, v132\n"
    "v_cndmask_b32 v151, 0, v153, vcc\n"
    "s_lshl_b32 s55, s44, 3\n"
    "v_xor_b32 v26, v26, v151\n"
    "v_xor_b32 v27, v27, v151\n"
    "v_xor_b32 v28, v28, v151\n"
    "v_xor_b32 v29, v29, v151\n"
    "v_lshl_add_u32 v150, v164, 2, s55\n"
    "v_xor_b32 v30, v26, v152\n"
    "v_xor_b32 v31, v27, v152\n"
    "v_xor_b32 v32, v28, v152\n"
    "v_xor_b32 v33, v29, v152\n"
    "v_add_u32 v150, v150, v149\n"
    ".Lk10:\n"
    "s_sub_u32 s55, 16, s44\n"
    "v_fmamk_f32 v142, v159, 0x3b800000, v158\n"
    "s_and_b32 s55, s55, 15\n"
    "v_fma_f32 v141, v143, v142, v161\n"
    "s_sub_i32 s55, s55, s44\n"
    "v_add_f32_e64 v137, v142, v141\n"
    "v_fma_f32 v134, v142, 2.0, v141\n"
    "v_fmamk_f32 v131, v142, 0x40400000, v141\n"
    "v_fmamk_f32 v128, v142, 0x41000000, v141\n"
    "v_fmamk_f32 v125, v142, 0x41100000, v141\n"
    "v_fmamk_f32 v122, v142, 0x41200000, v141\n"
    "v_fmamk_f32 v119, v142, 0x41300000, v141\n"
    "v_mul_i32_i24 v140, s55, v164\n"
    "v_cos_f32 v139, v141\n"
    "v_sin_f32 v138, v141\n"
    "v_cos_f32 v136, v137\n"
    "v_sin_f32 v135, v137\n"
    "v_cos_f32 v133, v134\n"
    "v_sin_f32 v132, v134\n"
    "v_cos_f32 v130, v131\n"
    "v_sin_f32 v129, v131\n"
    "v_cos_f32 v127, v128\n"
    "v_sin_f32 v126, v128\n"
    "v_cos_f32 v124, v125\n"
    "v_sin_f32 v123, v125\n"
    "v_cos_f32 v121, v122\n"
    "v_sin_f32 v120, v122\n"
    "v_cos_f32 v118, v119\n"
    "v_sin_f32 v117, v119\n"
    "v_add_u32 v140, s44, v140\n"
    "v_and_b32 v145, 8, v0\n"
    "v_lshl_add_u32 v148, v140, 4, v167\n"
    "v_lshl_add_u32 v145, v165, 2, v145\n"
    "v_and_b32 v163, 4, v0\n"
    "s_mul_i32 s57, s54, 17792\n"
    "s_lshl_b32 s56, s44, 7\n"
    "v_mul_u32_u24 v148, 80, v148\n"
    "v_lshl_add_u32 v145, v163, 2, v145\n"
    "s_add_u32 s57, s57, s56\n"
    "v_lshl_add_u32 v149, v140, 2, v149\n"
    "v_fmamk_f32 v146, v159, 0x3d800000, v160\n"
    "v_cvt_pk_bf16_f32 v34, v139, v138\n"
    "v_cvt_pk_bf16_f32 v35, v136, v135\n"
    "v_cvt_pk_bf16_f32 v36, v133, v132\n"
    "v_cvt_pk_bf16_f32 v37, v130, v129\n"
    "v_cvt_pk_bf16_f32 v38, v127, v126\n"
    "v_cvt_pk_bf16_f32 v39, v124, v123\n"
    "v_cvt_pk_bf16_f32 v40, v121, v120\n"
    "v_cvt_pk_bf16_f32 v41, v118, v117\n"
    "v_lshl_add_u32 v148, v165, 4, v148\n"
    "v_lshlrev_b32_e64 v147, 2, v1\n"
    "v_lshl_add_u32 v145, v164, 5, v145\n"
    "s_add_u32 s57, s57, 82048\n"
    "v_add_u32 v149, 52736, v149\n"
    "v_fma_f32 v146, v146, v158, v161\n"
    "s_cmp_ge_u32 s44, 8\n"
    "s_cbranch_scc1 .Lk9\n"
    "s_waitcnt vmcnt(4)\n"
    "v_mfma_f32_32x32x16_bf16 v[58:73], v[26:29], v[2:5], 0\n"
    "s_waitcnt vmcnt(0)\n"
    "v_mfma_f32_32x32x16_bf16 v[58:73], v[30:33], v[6:9], v[58:73]\n"
    "s_nop 0\n"
    "s_nop 4\n"
    "v_cvt_pk_bf16_f32 v58, v58, v59\n"
    "v_cvt_pk_bf16_f32 v60, v60, v61\n"
    "v_cvt_pk_bf16_f32 v62, v62, v63\n"
    "v_cvt_pk_bf16_f32 v64, v64, v65\n"
    "v_cvt_pk_bf16_f32 v66, v66, v67\n"
    "v_cvt_pk_bf16_f32 v68, v68, v69\n"
    "v_cvt_pk_bf16_f32 v70, v70, v71\n"
    "v_cvt_pk_bf16_f32 v72, v72, v73\n"
    "ds_write2st64_b32 v150, v58, v60 offset0:0 offset1:10\n"
    "ds_write2st64_b32 v150, v62, v64 offset0:20 offset1:30\n"
    "ds_write2st64_b32 v150, v66, v68 offset0:40 offset1:50\n"
    "ds_write2st64_b32 v150, v70, v72 offset0:60 offset1:70\n"
    "s_branch .Lk8\n"
    ".Lk7:\n"
    "s_cmp_eq_u32 s44, 11\n"
    "s_cbranch_scc1 .Lk11\n"
    "v_add_f32 v143, v159, v158\n"
    "v_add_f32 v143, 0xc1100000, v143\n"
    "v_mul_f32 v143, 0x3f638e39, v143\n"
    "v_fma_f32 v142, v160, v143, v161\n"
    "v_fma_f32 v138, v143, 2.0, v142\n"
    "v_fma_f32 v135, v143, 4.0, v142\n"
    "v_fmamk_f32 v132, v143, 0x40c00000, v142\n"
    "v_fmamk_f32 v129, v143, 0x41000000, v142\n"
    "v_fmamk_f32 v126, v143, 0x41200000, v142\n"
    "v_fmamk_f32 v123, v143, 0x41400000, v142\n"
    "v_fmamk_f32 v120, v143, 0x41600000, v142\n"
    "v_fmamk_f32 v117, v143, 0x41800000, v142\n"
    "s_cmp_eq_u32 s44, 10\n"
    "v_cos_f32 v140, v142\n"
    "v_sin_f32 v139, v142\n"
    "v_cos_f32 v137, v138\n"
    "v_sin_f32 v136, v138\n"
    "v_cos_f32 v134, v135\n"
    "v_sin_f32 v133, v135\n"
    "v_cos_f32 v131, v132\n"
    "v_sin_f32 v130, v132\n"
    "v_cos_f32 v128, v129\n"
    "v_sin_f32 v127, v129\n"
    "v_cos_f32 v125, v126\n"
    "v_sin_f32 v124, v126\n"
    "v_cos_f32 v122, v123\n"
    "v_sin_f32 v121, v123\n"
    "v_cos_f32 v119, v120\n"
    "v_sin_f32 v118, v120\n"
    "v_cos_f32 v116, v117\n"
    "v_sin_f32 v115, v117\n"
    "s_cselect_b32 s56, 2, 16\n"
    "v_cmp_gt_u32_e64 vcc, s56, v162\n"
    "v_cvt_pk_bf16_f32 v10, v140, v139\n"
    "v_cvt_pk_bf16_f32 v11, v137, v136\n"
    "v_cvt_pk_bf16_f32 v12, v134, v133\n"
    "v_cvt_pk_bf16_f32 v13, v131, v130\n"
    "v_cvt_pk_bf16_f32 v14, v128, v127\n"
    "v_cvt_pk_bf16_f32 v15, v125, v124\n"
    "v_cvt_pk_bf16_f32 v16, v122, v121\n"
    "v_cvt_pk_bf16_f32 v17, v119, v118\n"
    "v_cvt_pk_bf16_f32 v18, v116, v115\n"
    "s_lshl_b32 s55, s44, 11\n"
    "v_mov_b32 v19, 0\n"
    "v_cndmask_b32 v10, 0, v10, vcc\n"
    "v_cndmask_b32 v11, 0, v11, vcc\n"
    "v_cndmask_b32 v12, 0, v12, vcc\n"
    "v_cndmask_b32 v13, 0, v13, vcc\n"
    "v_cndmask_b32 v14, 0, v14, vcc\n"
    "v_cndmask_b32 v15, 0, v15, vcc\n"
    "v_cndmask_b32 v16, 0, v16, vcc\n"
    "v_cndmask_b32 v17, 0, v17, vcc\n"
    "v_cndmask_b32 v18, 0, v18, vcc\n"
    "v_add_u32 v141, s55, v155\n"
    "v_lshl_add_u32 v163, s44, 3, v155\n"
    "ds_write_b128 v141, v[10:13] offset:4608\n"
    "ds_write_b128 v141, v[14:17] offset:5632\n"
    "ds_write_b64 v163, v[18:19] offset:27064\n"
    "s_branch .Lk9\n"
    ".Lk11:\n"
    "v_cvt_f32_u32 v143, s51\n"
    "v_lshrrev_b32 v142, 1, v167\n"
    "v_mul_f32 v143, 0x3a000000, v143\n"
    "v_cvt_f32_u32 v142, v142\n"
    "v_mul_f32 v141, 0.5, v156\n"
    "v_fmamk_f32 v140, v142, 0x3e000000, v143\n"
    "v_mul_f32 v139, v141, v140\n"
    "v_cos_f32 v137, v139\n"
    "v_sin_f32 v136, v139\n"
    "v_add_f32 v135, v140, v139\n"
    "v_cos_f32 v134, v135\n"
    "v_sin_f32 v133, v135\n"
    "v_cvt_pk_bf16_f32 v10, v137, v136\n"
    "v_cvt_pk_bf16_f32 v14, -v136, v137\n"
    "v_mul_f32 v137, 0x3e000000, v142\n"
    "v_mul_f32 v136, v142, v143\n"
    "v_cvt_pk_bf16_f32 v12, v134, v133\n"
    "v_cvt_pk_bf16_f32 v16, -v133, v134\n"
    "v_fmac_f32 v136, v141, v137\n"
    "v_add_f32 v133, v137, v136\n"
    "v_cos_f32 v135, v136\n"
    "v_sin_f32 v134, v136\n"
    "v_cos_f32 v132, v133\n"
    "v_sin_f32 v131, v133\n"
    "v_mul_f32 v135, 0x35638e39, v135\n"
    "v_mul_f32 v134, 0x35638e39, v134\n"
    "v_mul_f32 v132, 0x35638e39, v132\n"
    "v_mul_f32 v131, 0x35638e39, v131\n"
    "v_bfe_i32 v138, v0, 0, 1\n"
    "v_cvt_pk_bf16_f32 v18, v135, v134\n"
    "v_cvt_pk_bf16_f32 v22, -v134, v135\n"
    "v_cvt_pk_bf16_f32 v20, v132, v131\n"
    "v_cvt_pk_bf16_f32 v24, -v131, v132\n"
    "v_and_b32 v11, v10, v138\n"
    "v_and_b32 v13, v12, v138\n"
    "v_and_b32 v15, v14, v138\n"
    "v_and_b32 v17, v16, v138\n"
    "v_and_b32 v19, v18, v138\n"
    "v_and_b32 v21, v20, v138\n"
    "v_and_b32 v23, v22, v138\n"
    "v_and_b32 v25, v24, v138\n"
    "v_xor_b32 v10, v10, v11\n"
    "v_xor_b32 v12, v12, v13\n"
    "v_xor_b32 v14, v14, v15\n"
    "v_xor_b32 v16, v16, v17\n"
    "v_xor_b32 v18, v18, v19\n"
    "v_xor_b32 v20, v20, v21\n"
    "v_xor_b32 v22, v22, v23\n"
    "v_xor_b32 v24, v24, v25\n"
    "ds_write_b128 v155, v[10:13] offset:29184\n"
    "ds_write_b128 v155, v[14:17] offset:30208\n"
    "ds_write_b128 v155, v[18:21] offset:31232\n"
    "ds_write_b128 v155, v[22:25] offset:32256\n"
    ".Lk9:\n"
    ".Lk8:\n"
    "s_waitcnt lgkmcnt(0)\n"
    "s_barrier\n"
    "s_cmp_ge_u32 s44, 9\n"
    "s_cbranch_scc1 .Lk6\n"
    "s_waitcnt vmcnt(4)\n"
    "ds_read_b128 v[2:5], v148\n"
    "s_waitcnt vmcnt(0)\n"
    "ds_read_b128 v[6:9], v148 offset:32\n"
    "s_and_b32 s55, s44, 7\n"
    "s_cbranch_scc1 .Lk13\n"
    "v_lshlrev_b32 v163, 31, v164\n"
    "s_waitcnt lgkmcnt(1)\n"
    "v_xor_b32 v2, v2, v163\n"
    "v_xor_b32 v3, v3, v163\n"
    "v_xor_b32 v4, v4, v163\n"
    "v_xor_b32 v5, v5, v163\n"
    "s_waitcnt lgkmcnt(0)\n"
    "v_xor_b32 v6, v6, v163\n"
    "v_xor_b32 v7, v7, v163\n"
    "v_xor_b32 v8, v8, v163\n"
    "v_xor_b32 v9, v9, v163\n"
    ".Lk13:\n"
    "s_waitcnt lgkmcnt(1)\n"
    "v_mfma_f32_32x32x16_bf16 v[58:73], v[34:37], v[2:5], 0\n"
    "s_waitcnt lgkmcnt(0)\n"
    "v_mfma_f32_32x32x16_bf16 v[58:73], v[38:41], v[6:9], v[58:73]\n"
    "s_nop 5\n"
    "v_cvt_pk_bf16_f32 v58, v58, v59\n"
    "v_cvt_pk_bf16_f32 v60, v60, v61\n"
    "v_cvt_pk_bf16_f32 v62, v62, v63\n"
    "v_cvt_pk_bf16_f32 v64, v64, v65\n"
    "v_and_b32 v58, -2, v58\n"
    "v_and_b32 v60, -2, v60\n"
    "v_and_b32 v62, -2, v62\n"
    "v_and_b32 v64, -2, v64\n"
    "buffer_store_dword v58, v147, s[16:19], s52 offen offset:0 sc1\n"
    "buffer_store_dword v60, v147, s[16:19], s52 offen offset:256 sc1\n"
    "buffer_store_dword v62, v147, s[16:19], s52 offen offset:512 sc1\n"
    "buffer_store_dword v64, v147, s[16:19], s52 offen offset:768 sc1\n"
    "s_cbranch_scc0 .Lk14\n"
    "v_cvt_pk_bf16_f32 v66, v66, v67\n"
    "v_cvt_pk_bf16_f32 v68, v68, v69\n"
    "v_cvt_pk_bf16_f32 v70, v70, v71\n"
    "v_cvt_pk_bf16_f32 v72, v72, v73\n"
    "v_and_b32 v66, -2, v66\n"
    "v_and_b32 v68, -2, v68\n"
    "v_and_b32 v70, -2, v70\n"
    "v_and_b32_e64 v72, -2, v72\n"
    "buffer_store_dword v66, v147, s[16:19], s52 offen offset:1024 sc1\n"
    "buffer_store_dword v68, v147, s[16:19], s52 offen offset:1280 sc1\n"
    "buffer_store_dword v70, v147, s[16:19], s52 offen offset:1536 sc1\n"
    "buffer_store_dword v72, v147, s[16:19], s52 offen offset:1792 sc1\n"
    ".Lk15:\n"
    "v_add_u32_dpp v59, v58, v58 row_ror:8 row_mask:0xf bank_mask:0xf bound_ctrl:1\n"
    "v_add_u32_dpp v61, v62, v62 row_ror:8 row_mask:0xf bank_mask:0xf bound_ctrl:1\n"
    "v_add_u32_dpp v63, v66, v66 row_ror:8 row_mask:0xf bank_mask:0xf bound_ctrl:1\n"
    "v_add_u32_dpp v65, v70, v70 row_ror:8 row_mask:0xf bank_mask:0xf bound_ctrl:1\n"
    "s_mul_i32 s53, s53, 1664525\n"
    "s_add_u32 s53, s53, 1013904223\n"
    "v_add_u32_dpp v59, v60, v60 row_ror:8 row_mask:0xf bank_mask:0xc\n"
    "v_add_u32_dpp v61, v64, v64 row_ror:8 row_mask:0xf bank_mask:0xc\n"
    "v_add_u32_dpp v63, v68, v68 row_ror:8 row_mask:0xf bank_mask:0xc\n"
    "v_add_u32_dpp v65, v72, v72 row_ror:8 row_mask:0xf bank_mask:0xc\n"
    "v_add_u32_dpp v67, v59, v59 row_half_mirror row_mask:0xf bank_mask:0xf bound_ctrl:1\n"
    "v_add_u32_dpp v69, v63, v63 row_half_mirror row_mask:0xf bank_mask:0xf bound_ctrl:1\n"
    "s_nop 0\n"
    "v_add_u32_dpp v67, v61, v61 row_half_mirror row_mask:0xf bank_mask:0xa\n"
    "v_add_u32_dpp v69, v65, v65 row_half_mirror row_mask:0xf bank_mask:0xa\n"
    "s_nop 1\n"
    "v_permlane16_swap_b32 v67, v69\n"
    "v_add_u32 v67, v67, v69\n"
    "s_nop 1\n"
    "v_add_u32_dpp v67, v67, v67 quad_perm:[1,0,3,2] row_mask:0xf bank_mask:0xf bound_ctrl:1\n"
    "s_nop 1\n"
    "v_add_u32_dpp v67, v67, v67 quad_perm:[2,3,0,1] row_mask:0xf bank_mask:0xf bound_ctrl:1\n"
    "v_add_u32 v67, s53, v67\n"
    "buffer_store_dword v67, v145, s[16:19], s57 offen sc1\n"
    "s_branch .Lk12\n"
    ".Lk14:\n"
    "s_cmp_eq_u32 s44, 8\n"
    "s_cbranch_scc1 .Lk15\n"
    "s_mov_b32 exec_hi, 0\n"
    "v_cvt_pk_bf16_f32 v66, v66, v67\n"
    "v_and_b32 v66, -2, v66\n"
    "buffer_store_dword v66, v147, s[16:19], s52 offen offset:1024 sc1\n"
    "s_mov_b64 exec, -1\n"
    "s_branch .Lk15\n"
    ".Lk6:\n"
    "s_mul_i32 s53, s53, 1664525\n"
    "s_add_u32 s53, s53, 1013904223\n"
    ".Lk12:\n"
    "s_setprio 0\n"
    "s_cmp_lt_i32 s50, 0\n"
    "s_cbranch_scc1 .Lk17\n"
    "s_cmp_ge_u32 s44, 8\n"
    "s_cbranch_scc1 .Lk18\n"
    "s_mul_i32 s57, s44, 320256\n"
    "s_lshl_b32 s56, s50, 7\n"
    "s_add_u32 s28, s57, s56\n"
    "s_nop 0\n"
    "s_add_u32 s28, s28, 65536\n"
    "s_add_u32 s29, s28, 35584\n"
    "s_add_u32 s30, s28, 71168\n"
    "s_add_u32 s31, s28, 106752\n"
    "s_add_u32 s32, s28, 142336\n"
    "s_add_u32 s33, s28, 177920\n"
    "s_add_u32 s34, s28, 213504\n"
    "s_add_u32 s35, s28, 249088\n"
    "s_add_u32 s36, s28, 284672\n"
    "s_and_b32 s55, s51, 15\n"
    "s_lshl_b32 s55, s55, 5\n"
    "s_lshr_b32 s56, s51, 4\n"
    "v_cmp_gt_u32 vcc, 72, v147\n"
    "s_add_u32 s55, s55, s56\n"
    "s_lshl_b32 s55, s55, 2\n"
    "v_mul_u32_u24 v1, 17664, v165\n"
    "v_mul_u32_u24 v145, 4448, v147\n"
    "s_add_u32 s57, s57, s55\n"
    "v_add_u32 v1, v1, v147\n"
    "v_cndmask_b32 v145, 2.0, v145, vcc\n"
    "s_add_u32 s57, s57, 82048\n"
    "v_mov_b32 v87, 0\n"
    "v_mov_b32 v88, 0\n"
    "v_mov_b32 v89, 0\n"
    "s_branch .Lk19\n"
    ".Lk18:\n"
    "s_cmp_eq_u32 s44, 8\n"
    "s_cselect_b32 s55, -1, s50\n"
    "s_cmp_lt_i32 s55, 0\n"
    "s_cbranch_scc1 .Lk20\n"
    "s_bitcmp1_b32 s44, 1\n"
    "s_cselect_b64 s[40:41], s[12:13], s[8:9]\n"
    "s_mov_b64 s[42:43], s[10:11]\n"
    "s_sub_u32 s55, 2048, s51\n"
    "s_mov_b32 s56, 147968\n"
    "s_bitcmp1_b32 s44, 0\n"
    "s_cselect_b32 s55, s55, s51\n"
    "s_cselect_b32 s56, -147968, s56\n"
    "s_and_b32 s37, s55, 2047\n"
    "s_mul_i32 s37, s37, 578\n"
    "s_and_b32 s37, s37, -16\n"
    "s_mul_i32 s55, s55, 578\n"
    "s_and_b32 s55, s55, -16\n"
    "s_mul_i32 m0, s44, 4736\n"
    "s_add_u32 m0, m0, -4608\n"
    "s_bfm_b64 exec, 37, 0\n"
    "buffer_load_dwordx4 v155, s[40:43], s37 offen lds\n"
    "s_add_u32 m0, m0, 592\n"
    "s_add_u32 s55, s55, s56\n"
    "buffer_load_dwordx4 v155, s[40:43], s55 offen lds\n"
    "s_add_u32 m0, m0, 592\n"
    "s_add_u32 s55, s55, s56\n"
    "buffer_load_dwordx4 v155, s[40:43], s55 offen lds\n"
    "s_add_u32 m0, m0, 592\n"
    "s_add_u32 s55, s55, s56\n"
    "buffer_load_dwordx4 v155, s[40:43], s55 offen lds\n"
    "s_add_u32 m0, m0, 592\n"
    "s_add_u32 s55, s55, s56\n"
    "buffer_load_dwordx4 v155, s[40:43], s55 offen lds\n"
    "s_add_u32 m0, m0, 592\n"
    "s_add_u32 s55, s55, s56\n"
    "buffer_load_dwordx4 v155, s[40:43], s55 offen lds\n"
    "s_add_u32 m0, m0, 592\n"
    "s_add_u32 s55, s55, s56\n"
    "buffer_load_dwordx4 v155, s[40:43], s55 offen lds\n"
    "s_add_u32 m0, m0, 592\n"
    "s_add_u32 s55, s55, s56\n"
    "buffer_load_dwordx4 v155, s[40:43], s55 offen lds\n"
    "s_sub_u32 s37, s44, 9\n"
    "s_mul_i32 s37, s37, 3\n"
    "s_min_u32 s37, s37, 5\n"
    "s_mul_i32 m0, s37, 592\n"
    "s_add_u32 m0, m0, 33280\n"
    "s_lshl_b32 s37, s37, 8\n"
    "s_add_u32 s37, s37, s51\n"
    "s_mul_i32 s37, s37, 578\n"
    "s_and_b32 s37, s37, -16\n"
    "buffer_load_dwordx4 v155, s[8:11], s37 offen lds\n"
    "s_add_u32 m0, m0, 592\n"
    "s_add_u32 s37, s37, 147968\n"
    "buffer_load_dwordx4 v155, s[8:11], s37 offen lds\n"
    "s_add_u32 m0, m0, 592\n"
    "s_add_u32 s37, s37, 147968\n"
    "buffer_load_dwordx4 v155, s[8:11], s37 offen lds\n"
    "s_mov_b64 exec, -1\n"
    ".Lk20:\n"
    ".Lk19:\n"
    "s_add_u32 s58, s44, 1\n"
    "s_cmp_lt_u32 s44, 6\n"
    "s_cselect_b32 s58, s58, s44\n"
    "s_cmp_eq_u32 s44, 6\n"
    "s_cselect_b32 s58, 0, s58\n"
    "v_and_b32 v157, 3, v0\n"
    "v_bfe_u32 v154, v0, 2, 2\n"
    "v_lshl_or_b32 v154, v157, 2, v154\n"
    "v_cvt_f32_u32 v142, s58\n"
    "v_cvt_f32_u32 v154, v154\n"
    "v_mul_f32 v142, 0x3b638e39, v142\n"
    "v_fmamk_f32 v143, v154, 0x3d800000, v142\n"
    "v_fmamk_f32 v144, v156, 0x3c800000, v142\n"
    "v_mul_f32 v142, v156, v143\n"
    "v_add_f32 v138, v143, v142\n"
    "v_cos_f32 v140, v142\n"
    "v_sin_f32 v139, v142\n"
    "v_cos_f32 v73, v138\n"
    "v_sin_f32 v72, v138\n"
    "v_cvt_f32_u32 v157, v167\n"
    "v_mul_f32 v141, v157, v144\n"
    "v_cvt_pk_bf16_f32 v42, v140, v139\n"
    "v_cvt_pk_bf16_f32 v46, -v139, v140\n"
    "v_cvt_pk_bf16_f32 v43, v73, v72\n"
    "v_cvt_pk_bf16_f32 v47, -v72, v73\n"
    "v_fmamk_f32 v138, v157, 0x3e800000, v141\n"
    "v_fma_f32 v71, v143, 2.0, v142\n"
    "v_fmamk_f32 v68, v143, 0x40400000, v142\n"
    "v_cos_f32 v140, v141\n"
    "v_sin_f32 v139, v141\n"
    "v_cos_f32 v73, v138\n"
    "v_sin_f32 v72, v138\n"
    "v_cos_f32 v70, v71\n"
    "v_sin_f32 v69, v71\n"
    "v_cos_f32 v67, v68\n"
    "v_sin_f32 v66, v68\n"
    "v_cvt_pk_bf16_f32 v50, v140, v139\n"
    "v_cvt_pk_bf16_f32 v54, -v139, v140\n"
    "v_cvt_pk_bf16_f32 v51, v73, v72\n"
    "v_cvt_pk_bf16_f32 v55, -v72, v73\n"
    "v_cvt_pk_bf16_f32 v44, v70, v69\n"
    "v_cvt_pk_bf16_f32 v48, -v69, v70\n"
    "v_cvt_pk_bf16_f32 v45, v67, v66\n"
    "v_cvt_pk_bf16_f32 v49, -v66, v67\n"
    "v_xor_b32 v52, v50, v153\n"
    "v_xor_b32 v56, v54, v153\n"
    "v_xor_b32 v53, v51, v153\n"
    "v_xor_b32 v57, v55, v153\n"
    "ds_read_b128 v[90:93], v155 offset:29184\n"
    "ds_read_b128 v[94:97], v155 offset:30208\n"
    "ds_read_b128 v[98:101], v155 offset:31232\n"
    "ds_read_b128 v[102:105], v155 offset:32256\n"
    "s_cmp_ge_u32 s44, 8\n"
    "s_cbranch_scc1 .Lk21\n"
    "ds_read_b128 v[38:41], v155 offset:23040\n"
    "ds_read_b128 v[74:77], v155 offset:24064\n"
    "ds_read_b128 v[78:81], v155 offset:25088\n"
    "ds_read_b128 v[82:85], v155 offset:26112\n"
    "ds_read_b128 v[34:37], v155 offset:27136\n"
    ".Lk21:\n"
    "s_lshl_b32 s55, s51, 1\n"
    "s_sub_u32 s56, 0, s55\n"
    "s_and_b32 s55, s55, 15\n"
    "s_and_b32 s56, s56, 15\n"
    "s_sub_u32 s59, s55, s56\n"
    "s_add_u32 s55, s55, s58\n"
    "s_add_u32 s55, s55, s58\n"
    "s_sub_u32 s56, s56, s58\n"
    "v_add_f32 v161, v161, v161\n"
    "s_sub_u32 s56, s56, s58\n"
    "v_and_b32 v160, 14, v0\n"
    "v_fma_f32 v144, v144, 0.5, v161\n"
    "v_mul_u32_u24 v157, 80, v167\n"
    "v_mul_u32_u24 v163, 36, v166\n"
    "v_mul_u32_u24 v160, 296, v160\n"
    "v_add_f32 v61, 0x3e000000, v144\n"
    "s_mul_i32 s58, s58, 1280\n"
    "v_lshl_add_u32 v157, v166, 4, v157\n"
    "s_add_u32 s56, s56, 4880\n"
    "v_add3_u32 v162, v160, v163, s55\n"
    "v_cos_f32 v59, v61\n"
    "v_add_u32 v154, s58, v157\n"
    "v_sub_u32 v160, v160, v163\n"
    "v_cos_f32 v58, v144\n"
    "v_sin_f32 v60, v144\n"
    "v_sin_f32 v61, v61\n"
    "s_sub_u32 s58, 23040, s58\n"
    "v_add_u32 v160, s56, v160\n"
    "v_add_u32 v157, s58, v157\n"
    "s_cmp_ge_u32 s44, 8\n"
    "s_cbranch_scc1 .Lk22\n"
    "s_waitcnt vmcnt(0)\n"
    ".Lk23:\n"
    "s_sleep 1\n"
    "s_memrealtime s[60:61]\n"
    "s_waitcnt lgkmcnt(0)\n"
    "s_sub_u32 s60, s60, s46\n"
    "s_cmp_lt_u32 s60, 235\n"
    "s_cbranch_scc1 .Lk23\n"
    "s_nop 0\n"
    "buffer_load_dword v2, v1, s[16:19], s28 offen sc1\n"
    "buffer_load_dword v3, v1, s[16:19], s29 offen sc1\n"
    "buffer_load_dword v4, v1, s[16:19], s30 offen sc1\n"
    "buffer_load_dword v5, v1, s[16:19], s31 offen sc1\n"
    "buffer_load_dword v6, v1, s[16:19], s32 offen sc1\n"
    "buffer_load_dword v7, v1, s[16:19], s33 offen sc1\n"
    "buffer_load_dword v8, v1, s[16:19], s34 offen sc1\n"
    "buffer_load_dword v9, v1, s[16:19], s35 offen sc1\n"
    "buffer_load_dword v86, v1, s[16:19], s36 offen sc1\n"
    "buffer_load_dword v148, v145, s[16:19], s57 offen sc1\n"
    ".Lk22:\n"
    "s_cmp_ge_u32 s44, 8\n"
    "s_cbranch_scc0 .Lk24\n"
    "s_waitcnt vmcnt(0)\n"
    ".Lk24:\n"
    "s_waitcnt lgkmcnt(0)\n"
    "s_barrier\n"
    "s_nop 0\n"
    "ds_read_u16_d16_hi v106, v162 offset:33280\n"
    "ds_read_u16_d16_hi v118, v160 offset:33712\n"
    "ds_read_u16_d16_hi v110, v162 offset:42752\n"
    "ds_read_u16_d16_hi v114, v160 offset:43184\n"
    "ds_read_u16_d16_hi v107, v162 offset:33424\n"
    "ds_read_u16_d16_hi v119, v160 offset:33568\n"
    "ds_read_u16_d16_hi v111, v162 offset:42896\n"
    "ds_read_u16_d16_hi v115, v160 offset:43040\n"
    "ds_read_u16_d16_hi v108, v162 offset:33568\n"
    "ds_read_u16_d16_hi v120, v160 offset:33424\n"
    "ds_read_u16_d16_hi v112, v162 offset:43040\n"
    "ds_read_u16_d16_hi v116, v160 offset:42896\n"
    "ds_read_u16_d16_hi v109, v162 offset:33712\n"
    "ds_read_u16_d16_hi v121, v160 offset:33280\n"
    "ds_read_u16_d16_hi v113, v162 offset:43184\n"
    "ds_read_u16_d16_hi v117, v160 offset:42752\n"
    "s_cmp_ge_u32 s44, 6\n"
    "s_cbranch_scc1 .Lk25\n"
    "v_add_u32 v160, s59, v160\n"
    "v_subrev_u32 v162, s59, v162\n"
    "ds_read_u16_d16_hi v122, v160 offset:28976\n"
    "ds_read_u16_d16_hi v134, v162 offset:38016\n"
    "ds_read_u16_d16_hi v126, v160 offset:38448\n"
    "ds_read_u16_d16_hi v130, v162 offset:47488\n"
    "ds_read_u16_d16_hi v123, v160 offset:28832\n"
    "ds_read_u16_d16_hi v135, v162 offset:38160\n"
    "ds_read_u16_d16_hi v127, v160 offset:38304\n"
    "ds_read_u16_d16_hi v131, v162 offset:47632\n"
    "ds_read_u16_d16_hi v124, v160 offset:28688\n"
    "ds_read_u16_d16_hi v136, v162 offset:38304\n"
    "ds_read_u16_d16_hi v128, v160 offset:38160\n"
    "ds_read_u16_d16_hi v132, v162 offset:47776\n"
    "ds_read_u16_d16_hi v125, v160 offset:28544\n"
    "ds_read_u16_d16_hi v137, v162 offset:38448\n"
    "ds_read_u16_d16_hi v129, v160 offset:38016\n"
    "ds_read_u16_d16_hi v133, v162 offset:47920\n"
    ".Lk25:\n"
    "s_cmp_eq_u32 s44, 6\n"
    "s_cbranch_scc0 .Lk26\n"
    "s_mov_b64 exec, 65535\n"
    "ds_read_u16_d16_hi v62, v160 offset:33136\n"
    "ds_read_u16_d16_hi v63, v160 offset:42608\n"
    "ds_read_u16_d16_hi v64, v162 offset:33856\n"
    "ds_read_u16_d16_hi v65, v162 offset:43328\n"
    "s_waitcnt lgkmcnt(3)\n"
    "v_mul_f32 v62, 0.5, v62\n"
    "v_fma_f32 v106, v106, 0.5, v62\n"
    "s_waitcnt lgkmcnt(2)\n"
    "v_mul_f32 v63, -0.5, v63\n"
    "v_fma_f32 v110, v110, 0.5, v63\n"
    "s_waitcnt lgkmcnt(1)\n"
    "v_mul_f32 v64, 0.5, v64\n"
    "v_fma_f32 v118, v118, 0.5, v64\n"
    "s_waitcnt lgkmcnt(0)\n"
    "v_mul_f32 v65, -0.5, v65\n"
    "v_fma_f32 v114, v114, 0.5, v65\n"
    "s_mov_b64 exec, -1\n"
    ".Lk26:\n"
    "s_waitcnt lgkmcnt(10)\n"
    "v_pk_add_f32 v[62:63], v[106:107], v[118:119] neg_lo:[0,1] neg_hi:[0,1]\n"
    "v_pk_add_f32 v[106:107], v[106:107], v[118:119]\n"
    "s_waitcnt lgkmcnt(8)\n"
    "v_pk_add_f32 v[64:65], v[110:111], v[114:115]\n"
    "v_pk_add_f32 v[110:111], v[110:111], v[114:115] neg_lo:[0,1] neg_hi:[0,1]\n"
    "v_pk_mul_f32 v[118:119], v[58:59], v[62:63]\n"
    "v_pk_mul_f32 v[114:115], v[58:59], v[64:65] neg_lo:[1,0] neg_hi:[1,0]\n"
    "v_pk_fma_f32 v[106:107], v[60:61], v[62:63], v[106:107] neg_lo:[1,0,0] neg_hi:[1,0,0]\n"
    "v_pk_fma_f32 v[110:111], v[60:61], v[64:65], v[110:111] neg_lo:[1,0,0] neg_hi:[1,0,0]\n"
    "s_waitcnt lgkmcnt(2)\n"
    "v_pk_add_f32 v[62:63], v[108:109], v[120:121] neg_lo:[0,1] neg_hi:[0,1]\n"
    "v_pk_add_f32 v[108:109], v[108:109], v[120:121]\n"
    "s_waitcnt lgkmcnt(0)\n"
    "v_pk_add_f32 v[64:65], v[112:113], v[116:117]\n"
    "v_pk_add_f32 v[112:113], v[112:113], v[116:117] neg_lo:[0,1] neg_hi:[0,1]\n"
    "v_pk_mul_f32 v[120:121], v[60:61], v[62:63] neg_lo:[1,0] neg_hi:[1,0]\n"
    "v_pk_mul_f32 v[116:117], v[60:61], v[64:65]\n"
    "v_pk_fma_f32 v[108:109], v[58:59], v[62:63], v[108:109] neg_lo:[1,0,0] neg_hi:[1,0,0]\n"
    "v_pk_fma_f32 v[112:113], v[58:59], v[64:65], v[112:113] neg_lo:[1,0,0] neg_hi:[1,0,0]\n"
    "s_cmp_ge_u32 s44, 6\n"
    "s_cbranch_scc1 .Lk27\n"
    "s_nop 0\n"
    "v_pk_add_f32 v[62:63], v[122:123], v[134:135] neg_lo:[0,1] neg_hi:[0,1]\n"
    "v_pk_add_f32 v[122:123], v[122:123], v[134:135]\n"
    "v_pk_add_f32 v[64:65], v[126:127], v[130:131]\n"
    "v_pk_add_f32 v[126:127], v[126:127], v[130:131] neg_lo:[0,1] neg_hi:[0,1]\n"
    "v_pk_mul_f32 v[134:135], v[58:59], v[62:63] neg_lo:[1,0] neg_hi:[1,0]\n"
    "v_pk_mul_f32 v[130:131], v[58:59], v[64:65]\n"
    "v_pk_fma_f32 v[122:123], v[60:61], v[62:63], v[122:123] neg_lo:[1,0,0] neg_hi:[1,0,0]\n"
    "v_pk_fma_f32 v[126:127], v[60:61], v[64:65], v[126:127] neg_lo:[1,0,0] neg_hi:[1,0,0]\n"
    "v_pk_add_f32 v[62:63], v[124:125], v[136:137] neg_lo:[0,1] neg_hi:[0,1]\n"
    "v_pk_add_f32 v[124:125], v[124:125], v[136:137]\n"
    "v_pk_add_f32 v[64:65], v[128:129], v[132:133]\n"
    "v_pk_add_f32 v[128:129], v[128:129], v[132:133] neg_lo:[0,1] neg_hi:[0,1]\n"
    "v_pk_mul_f32 v[136:137], v[60:61], v[62:63]\n"
    "v_pk_mul_f32 v[132:133], v[60:61], v[64:65] neg_lo:[1,0] neg_hi:[1,0]\n"
    "v_pk_fma_f32 v[124:125], v[58:59], v[62:63], v[124:125] neg_lo:[1,0,0] neg_hi:[1,0,0]\n"
    "v_pk_fma_f32 v[128:129], v[58:59], v[64:65], v[128:129] neg_lo:[1,0,0] neg_hi:[1,0,0]\n"
    ".Lk27:\n"
    "s_cmp_ge_u32 s44, 8\n"
    "s_cbranch_scc1 .Lk28\n"
    "s_mul_i32 s56, s53, 18\n"
    "s_waitcnt vmcnt(5)\n"
    "v_add3_u32 v163, v2, v3, v4\n"
    "s_waitcnt vmcnt(3)\n"
    "v_add3_u32 v163, v163, v5, v6\n"
    "s_waitcnt vmcnt(1)\n"
    "v_add3_u32 v163, v163, v7, v8\n"
    "s_waitcnt vmcnt(0)\n"
    "v_add3_u32 v163, v163, v9, v86\n"
    "v_sub_u32 v163, v148, v163\n"
    "s_nop 1\n"
    "v_add_u32_dpp v163, v163, v163 quad_perm:[1,0,3,2] row_mask:0xf bank_mask:0xf bound_ctrl:1\n"
    "s_nop 1\n"
    "v_add_u32_dpp v163, v163, v163 quad_perm:[2,3,0,1] row_mask:0xf bank_mask:0xf bound_ctrl:1\n"
    "s_nop 1\n"
    "v_add_u32_dpp v163, v163, v163 row_half_mirror row_mask:0xf bank_mask:0xf bound_ctrl:1\n"
    "s_nop 1\n"
    "v_add_u32_dpp v163, v163, v163 row_mirror row_mask:0xf bank_mask:0xf bound_ctrl:1\n"
    "s_nop 1\n"
    "v_add_u32_dpp v163, v163, v163 row_bcast:15 row_mask:0xa bank_mask:0xf\n"
    "s_nop 1\n"
    "v_add_u32_dpp v163, v163, v163 row_bcast:31 row_mask:0xc bank_mask:0xf\n"
    "v_cmp_ne_u32_e64 vcc, s56, v163\n"
    "s_lshr_b64 vcc, vcc, 63\n"
    "s_cbranch_vccz .Lk30\n"
    "s_mov_b32 s58, 32768\n"
    ".Lk31:\n"
    "buffer_load_dword v2, v1, s[16:19], s28 offen sc1\n"
    "buffer_load_dword v3, v1, s[16:19], s29 offen sc1\n"
    "buffer_load_dword v4, v1, s[16:19], s30 offen sc1\n"
    "buffer_load_dword v5, v1, s[16:19], s31 offen sc1\n"
    "buffer_load_dword v6, v1, s[16:19], s32 offen sc1\n"
    "buffer_load_dword v7, v1, s[16:19], s33 offen sc1\n"
    "buffer_load_dword v8, v1, s[16:19], s34 offen sc1\n"
    "buffer_load_dword v9, v1, s[16:19], s35 offen sc1\n"
    "buffer_load_dword v86, v1, s[16:19], s36 offen sc1\n"
    "buffer_load_dword v148, v145, s[16:19], s57 offen sc1\n"
    "s_waitcnt vmcnt(7)\n"
    "v_add3_u32 v163, v2, v3, v4\n"
    "s_waitcnt vmcnt(5)\n"
    "v_add3_u32 v163, v163, v5, v6\n"
    "s_waitcnt vmcnt(3)\n"
    "v_add3_u32 v163, v163, v7, v8\n"
    "s_waitcnt vmcnt(1)\n"
    "v_add3_u32 v163, v163, v9, v86\n"
    "s_waitcnt vmcnt(0)\n"
    "v_sub_u32 v163, v148, v163\n"
    "s_nop 1\n"
    "v_add_u32_dpp v163, v163, v163 quad_perm:[1,0,3,2] row_mask:0xf bank_mask:0xf bound_ctrl:1\n"
    "s_nop 1\n"
    "v_add_u32_dpp v163, v163, v163 quad_perm:[2,3,0,1] row_mask:0xf bank_mask:0xf bound_ctrl:1\n"
    "s_nop 1\n"
    "v_add_u32_dpp v163, v163, v163 row_half_mirror row_mask:0xf bank_mask:0xf bound_ctrl:1\n"
    "s_nop 1\n"
    "v_add_u32_dpp v163, v163, v163 row_mirror row_mask:0xf bank_mask:0xf bound_ctrl:1\n"
    "s_nop 1\n"
    "v_add_u32_dpp v163, v163, v163 row_bcast:15 row_mask:0xa bank_mask:0xf\n"
    "s_nop 1\n"
    "v_add_u32_dpp v163, v163, v163 row_bcast:31 row_mask:0xc bank_mask:0xf\n"
    "v_cmp_ne_u32_e64 vcc, s56, v163\n"
    "s_lshr_b64 vcc, vcc, 63\n"
    "s_cbranch_vccz .Lk32\n"
    "s_sub_u32 s58, s58, 1\n"
    "s_cbranch_scc0 .Lk31\n"
    ".Lk32:\n"
    ".Lk30:\n"
    "v_mfma_f32_32x32x16_bf16 v[58:73], v[38:41], v[2:5], 0\n"
    "v_mfma_f32_32x32x16_bf16 v[58:73], v[74:77], v[6:9], v[58:73]\n"
    "v_mfma_f32_32x32x16_bf16 v[58:73], v[34:37], v[86:89], v[58:73]\n"
    "v_mfma_f32_32x32x16_bf16 v[10:25], v[78:81], v[2:5], 0\n"
    "v_cvt_pk_bf16_f32 v58, v58, v59\n"
    "v_cvt_pk_bf16_f32 v60, v60, v61\n"
    "v_cvt_pk_bf16_f32 v62, v62, v63\n"
    "v_cvt_pk_bf16_f32 v64, v64, v65\n"
    "v_cvt_pk_bf16_f32 v66, v66, v67\n"
    "v_cvt_pk_bf16_f32 v68, v68, v69\n"
    "v_mfma_f32_32x32x16_bf16 v[10:25], v[82:85], v[6:9], v[10:25]\n"
    "ds_write2st64_b32 v150, v58, v60 offset0:0 offset1:10\n"
    "ds_write2st64_b32 v150, v62, v64 offset0:20 offset1:30\n"
    "ds_write2st64_b32 v150, v66, v68 offset0:40 offset1:50\n"
    "v_cvt_pk_bf16_f32 v70, v70, v71\n"
    "v_cvt_pk_bf16_f32 v72, v72, v73\n"
    "v_mfma_f32_32x32x16_bf16 v[10:25], v[36:39], v[86:89], v[10:25]\n"
    "ds_write2st64_b32 v150, v70, v72 offset0:60 offset1:70\n"
    "s_nop 4\n"
    "v_cvt_pk_bf16_f32 v10, v10, v11\n"
    "s_nop 4\n"
    "ds_write_b32 v150, v10 offset:20480\n"
    ".Lk28:\n"
    "s_waitcnt lgkmcnt(0)\n"
    "s_barrier\n"
    "s_cmp_ge_u32 s44, 6\n"
    "s_cbranch_scc1 .Lk33\n"
    "s_waitcnt vmcnt(4)\n"
    "ds_read_b128 v[2:5], v154\n"
    "s_waitcnt vmcnt(0)\n"
    "ds_read_b128 v[6:9], v157\n"
    "s_waitcnt lgkmcnt(1)\n"
    "v_mfma_f32_16x16x32_bf16 v[58:61], v[2:5], v[90:93], 0\n"
    "v_mfma_f32_16x16x32_bf16 v[62:65], v[2:5], v[94:97], 0\n"
    "s_waitcnt lgkmcnt(0)\n"
    "v_mfma_f32_16x16x32_bf16 v[66:69], v[6:9], v[90:93], 0\n"
    "v_mfma_f32_16x16x32_bf16 v[70:73], v[6:9], v[94:97], 0\n"
    "v_cvt_pk_bf16_f32 v2, v58, v62\n"
    "v_cvt_pk_bf16_f32 v3, v59, v63\n"
    "v_cvt_pk_bf16_f32 v4, v60, v64\n"
    "v_cvt_pk_bf16_f32 v5, v61, v65\n"
    "s_nop 0\n"
    "s_nop 0\n"
    "v_mfma_f32_16x16x32_bf16 v[58:61], v[42:45], v[2:5], 0\n"
    "v_mfma_f32_16x16x32_bf16 v[62:65], v[46:49], v[2:5], 0\n"
    "v_cvt_pk_bf16_f32 v6, v70, v66\n"
    "v_cvt_pk_bf16_f32 v7, v71, v67\n"
    "v_cvt_pk_bf16_f32 v8, v72, v68\n"
    "v_cvt_pk_bf16_f32 v9, v73, v69\n"
    "v_pk_mul_f32 v[14:15], v[106:107], v[58:59]\n"
    "v_pk_mul_f32 v[16:17], v[110:111], v[58:59]\n"
    "v_mfma_f32_16x16x32_bf16 v[66:69], v[46:49], v[6:9], 0\n"
    "v_mov_b32_dpp v10, v58 quad_perm:[1,0,3,2] row_mask:0xf bank_mask:0xf bound_ctrl:1\n"
    "v_mov_b32_dpp v11, v59 quad_perm:[1,0,3,2] row_mask:0xf bank_mask:0xf bound_ctrl:1\n"
    "v_mfma_f32_16x16x32_bf16 v[70:73], v[42:45], v[6:9], 0\n"
    "v_pk_fma_f32 v[14:15], v[114:115], v[10:11], v[14:15]\n"
    "v_pk_fma_f32 v[16:17], v[118:119], v[10:11], v[16:17]\n"
    "v_pk_fma_f32 v[14:15], v[110:111], v[62:63], v[14:15] neg_lo:[1,0,0] neg_hi:[1,0,0]\n"
    "v_pk_fma_f32 v[16:17], v[106:107], v[62:63], v[16:17]\n"
    "v_mov_b32_dpp v12, v62 quad_perm:[1,0,3,2] row_mask:0xf bank_mask:0xf bound_ctrl:1\n"
    "v_mov_b32_dpp v13, v63 quad_perm:[1,0,3,2] row_mask:0xf bank_mask:0xf bound_ctrl:1\n"
    "v_pk_fma_f32 v[14:15], v[118:119], v[12:13], v[14:15] neg_lo:[1,0,0] neg_hi:[1,0,0]\n"
    "v_pk_fma_f32 v[16:17], v[114:115], v[12:13], v[16:17]\n"
    "v_cvt_pk_bf16_f32 v2, v16, v14\n"
    "v_cvt_pk_bf16_f32 v3, v17, v15\n"
    "v_pk_mul_f32 v[14:15], v[108:109], v[60:61]\n"
    "v_pk_mul_f32 v[16:17], v[112:113], v[60:61]\n"
    "v_mov_b32_dpp v10, v60 quad_perm:[1,0,3,2] row_mask:0xf bank_mask:0xf bound_ctrl:1\n"
    "v_mov_b32_dpp v11, v61 quad_perm:[1,0,3,2] row_mask:0xf bank_mask:0xf bound_ctrl:1\n"
    "v_pk_fma_f32 v[14:15], v[116:117], v[10:11], v[14:15]\n"
    "v_pk_fma_f32 v[16:17], v[120:121], v[10:11], v[16:17]\n"
    "v_pk_fma_f32 v[14:15], v[112:113], v[64:65], v[14:15] neg_lo:[1,0,0] neg_hi:[1,0,0]\n"
    "v_pk_fma_f32 v[16:17], v[108:109], v[64:65], v[16:17]\n"
    "v_mov_b32_dpp v12, v64 quad_perm:[1,0,3,2] row_mask:0xf bank_mask:0xf bound_ctrl:1\n"
    "v_mov_b32_dpp v13, v65 quad_perm:[1,0,3,2] row_mask:0xf bank_mask:0xf bound_ctrl:1\n"
    "v_pk_fma_f32 v[14:15], v[120:121], v[12:13], v[14:15] neg_lo:[1,0,0] neg_hi:[1,0,0]\n"
    "v_pk_fma_f32 v[16:17], v[116:117], v[12:13], v[16:17]\n"
    "v_cvt_pk_bf16_f32 v4, v16, v14\n"
    "v_cvt_pk_bf16_f32 v5, v17, v15\n"
    "v_pk_mul_f32 v[22:23], v[122:123], v[66:67]\n"
    "v_pk_mul_f32 v[24:25], v[126:127], v[66:67]\n"
    "v_mfma_f32_16x16x32_bf16 v[58:61], v[2:5], v[50:53], 0\n"
    "v_mov_b32_dpp v18, v66 quad_perm:[1,0,3,2] row_mask:0xf bank_mask:0xf bound_ctrl:1\n"
    "v_mov_b32_dpp v19, v67 quad_perm:[1,0,3,2] row_mask:0xf bank_mask:0xf bound_ctrl:1\n"
    "v_mfma_f32_16x16x32_bf16 v[62:65], v[2:5], v[54:57], 0\n"
    "v_pk_fma_f32 v[22:23], v[130:131], v[18:19], v[22:23]\n"
    "v_pk_fma_f32 v[24:25], v[134:135], v[18:19], v[24:25]\n"
    "v_pk_fma_f32 v[22:23], v[126:127], v[70:71], v[22:23] neg_lo:[1,0,0] neg_hi:[1,0,0]\n"
    "v_pk_fma_f32 v[24:25], v[122:123], v[70:71], v[24:25]\n"
    "v_mov_b32_dpp v20, v70 quad_perm:[1,0,3,2] row_mask:0xf bank_mask:0xf bound_ctrl:1\n"
    "v_mov_b32_dpp v21, v71 quad_perm:[1,0,3,2] row_mask:0xf bank_mask:0xf bound_ctrl:1\n"
    "v_pk_fma_f32 v[22:23], v[134:135], v[20:21], v[22:23] neg_lo:[1,0,0] neg_hi:[1,0,0]\n"
    "v_pk_fma_f32 v[24:25], v[130:131], v[20:21], v[24:25]\n"
    "v_cvt_pk_bf16_f32 v6, v22, v24\n"
    "v_cvt_pk_bf16_f32 v7, v23, v25\n"
    "v_pk_mul_f32 v[22:23], v[124:125], v[68:69]\n"
    "v_pk_mul_f32 v[24:25], v[128:129], v[68:69]\n"
    "v_mov_b32_dpp v18, v68 quad_perm:[1,0,3,2] row_mask:0xf bank_mask:0xf bound_ctrl:1\n"
    "v_mov_b32_dpp v19, v69 quad_perm:[1,0,3,2] row_mask:0xf bank_mask:0xf bound_ctrl:1\n"
    "v_pk_fma_f32 v[22:23], v[132:133], v[18:19], v[22:23]\n"
    "v_pk_fma_f32 v[24:25], v[136:137], v[18:19], v[24:25]\n"
    "v_pk_fma_f32 v[22:23], v[128:129], v[72:73], v[22:23] neg_lo:[1,0,0] neg_hi:[1,0,0]\n"
    "v_pk_fma_f32 v[24:25], v[124:125], v[72:73], v[24:25]\n"
    "v_mov_b32_dpp v20, v72 quad_perm:[1,0,3,2] row_mask:0xf bank_mask:0xf bound_ctrl:1\n"
    "v_mov_b32_dpp v21, v73 quad_perm:[1,0,3,2] row_mask:0xf bank_mask:0xf bound_ctrl:1\n"
    "v_pk_fma_f32 v[22:23], v[136:137], v[20:21], v[22:23] neg_lo:[1,0,0] neg_hi:[1,0,0]\n"
    "v_pk_fma_f32 v[24:25], v[132:133], v[20:21], v[24:25]\n"
    "v_cvt_pk_bf16_f32 v8, v22, v24\n"
    "v_cvt_pk_bf16_f32 v9, v23, v25\n"
    "v_cvt_pk_bf16_f32 v2, v58, v62\n"
    "v_cvt_pk_bf16_f32 v3, v59, v63\n"
    "v_mfma_f32_16x16x32_bf16 v[66:69], v[6:9], v[50:53], 0\n"
    "v_cvt_pk_bf16_f32 v4, v60, v64\n"
    "v_cvt_pk_bf16_f32 v5, v61, v65\n"
    "v_mfma_f32_16x16x32_bf16 v[70:73], v[6:9], v[54:57], 0\n"
    "v_mfma_f32_16x16x32_bf16 v[58:61], v[98:101], v[2:5], 0\n"
    "v_mfma_f32_16x16x32_bf16 v[62:65], v[102:105], v[2:5], 0\n"
    "v_cvt_pk_bf16_f32 v6, v70, v66\n"
    "v_cvt_pk_bf16_f32 v7, v71, v67\n"
    "v_cvt_pk_bf16_f32 v8, v72, v68\n"
    "v_cvt_pk_bf16_f32 v9, v73, v69\n"
    "s_nop 0\n"
    "s_nop 0\n"
    "v_mfma_f32_16x16x32_bf16 v[66:69], v[98:101], v[6:9], 0\n"
    "v_mfma_f32_16x16x32_bf16 v[70:73], v[102:105], v[6:9], 0\n"
    "v_cvt_pk_bf16_f32 v2, v58, v62\n"
    "v_cvt_pk_bf16_f32 v3, v59, v63\n"
    "v_cvt_pk_bf16_f32 v4, v60, v64\n"
    "v_cvt_pk_bf16_f32 v5, v61, v65\n"
    "ds_write_b128 v154, v[2:5]\n"
    "s_nop 0\n"
    "s_nop 1\n"
    "v_cvt_pk_bf16_f32 v6, v66, v70\n"
    "v_cvt_pk_bf16_f32 v7, v67, v71\n"
    "v_cvt_pk_bf16_f32 v8, v68, v72\n"
    "v_cvt_pk_bf16_f32 v9, v69, v73\n"
    "ds_write_b128 v157, v[6:9]\n"
    "s_branch .Lk34\n"
    ".Lk33:\n"
    "s_setprio 1\n"
    "s_waitcnt vmcnt(4)\n"
    "ds_read_b128 v[2:5], v154\n"
    "s_waitcnt lgkmcnt(0)\n"
    "v_mfma_f32_16x16x32_bf16 v[58:61], v[2:5], v[90:93], 0\n"
    "v_mfma_f32_16x16x32_bf16 v[62:65], v[2:5], v[94:97], 0\n"
    "s_nop 0\n"
    "s_nop 6\n"
    "v_cvt_pk_bf16_f32 v2, v58, v62\n"
    "v_cvt_pk_bf16_f32 v3, v59, v63\n"
    "v_cvt_pk_bf16_f32 v4, v60, v64\n"
    "v_cvt_pk_bf16_f32 v5, v61, v65\n"
    "s_nop 1\n"
    "v_mfma_f32_16x16x32_bf16 v[58:61], v[42:45], v[2:5], 0\n"
    "v_mfma_f32_16x16x32_bf16 v[62:65], v[46:49], v[2:5], 0\n"
    "s_nop 3\n"
    "v_pk_mul_f32 v[14:15], v[106:107], v[58:59]\n"
    "v_pk_mul_f32 v[16:17], v[110:111], v[58:59]\n"
    "v_mov_b32_dpp v10, v58 quad_perm:[1,0,3,2] row_mask:0xf bank_mask:0xf bound_ctrl:1\n"
    "v_mov_b32_dpp v11, v59 quad_perm:[1,0,3,2] row_mask:0xf bank_mask:0xf bound_ctrl:1\n"
    "v_pk_fma_f32 v[14:15], v[114:115], v[10:11], v[14:15]\n"
    "v_pk_fma_f32 v[16:17], v[118:119], v[10:11], v[16:17]\n"
    "v_pk_fma_f32 v[14:15], v[110:111], v[62:63], v[14:15] neg_lo:[1,0,0] neg_hi:[1,0,0]\n"
    "v_pk_fma_f32 v[16:17], v[106:107], v[62:63], v[16:17]\n"
    "v_mov_b32_dpp v12, v62 quad_perm:[1,0,3,2] row_mask:0xf bank_mask:0xf bound_ctrl:1\n"
    "v_mov_b32_dpp v13, v63 quad_perm:[1,0,3,2] row_mask:0xf bank_mask:0xf bound_ctrl:1\n"
    "v_pk_fma_f32 v[14:15], v[118:119], v[12:13], v[14:15] neg_lo:[1,0,0] neg_hi:[1,0,0]\n"
    "v_pk_fma_f32 v[16:17], v[114:115], v[12:13], v[16:17]\n"
    "v_cvt_pk_bf16_f32 v2, v16, v14\n"
    "v_cvt_pk_bf16_f32 v3, v17, v15\n"
    "v_pk_mul_f32 v[14:15], v[108:109], v[60:61]\n"
    "v_pk_mul_f32 v[16:17], v[112:113], v[60:61]\n"
    "v_mov_b32_dpp v10, v60 quad_perm:[1,0,3,2] row_mask:0xf bank_mask:0xf bound_ctrl:1\n"
    "v_mov_b32_dpp v11, v61 quad_perm:[1,0,3,2] row_mask:0xf bank_mask:0xf bound_ctrl:1\n"
    "v_pk_fma_f32 v[14:15], v[116:117], v[10:11], v[14:15]\n"
    "v_pk_fma_f32 v[16:17], v[120:121], v[10:11], v[16:17]\n"
    "v_pk_fma_f32 v[14:15], v[112:113], v[64:65], v[14:15] neg_lo:[1,0,0] neg_hi:[1,0,0]\n"
    "v_pk_fma_f32 v[16:17], v[108:109], v[64:65], v[16:17]\n"
    "v_mov_b32_dpp v12, v64 quad_perm:[1,0,3,2] row_mask:0xf bank_mask:0xf bound_ctrl:1\n"
    "v_mov_b32_dpp v13, v65 quad_perm:[1,0,3,2] row_mask:0xf bank_mask:0xf bound_ctrl:1\n"
    "v_pk_fma_f32 v[14:15], v[120:121], v[12:13], v[14:15] neg_lo:[1,0,0] neg_hi:[1,0,0]\n"
    "v_pk_fma_f32 v[16:17], v[116:117], v[12:13], v[16:17]\n"
    "v_cvt_pk_bf16_f32 v4, v16, v14\n"
    "v_cvt_pk_bf16_f32 v5, v17, v15\n"
    "s_nop 1\n"
    "v_mfma_f32_16x16x32_bf16 v[58:61], v[2:5], v[50:53], 0\n"
    "v_mfma_f32_16x16x32_bf16 v[62:65], v[2:5], v[54:57], 0\n"
    "s_nop 7\n"
    "v_cvt_pk_bf16_f32 v2, v58, v62\n"
    "v_cvt_pk_bf16_f32 v3, v59, v63\n"
    "v_cvt_pk_bf16_f32 v4, v60, v64\n"
    "v_cvt_pk_bf16_f32 v5, v61, v65\n"
    "s_nop 1\n"
    "v_mfma_f32_16x16x32_bf16 v[58:61], v[98:101], v[2:5], 0\n"
    "v_mfma_f32_16x16x32_bf16 v[62:65], v[102:105], v[2:5], 0\n"
    "s_nop 7\n"
    "v_cvt_pk_bf16_f32 v2, v58, v62\n"
    "v_cvt_pk_bf16_f32 v3, v59, v63\n"
    "v_cvt_pk_bf16_f32 v4, v60, v64\n"
    "v_cvt_pk_bf16_f32 v5, v61, v65\n"
    "ds_write_b128 v154, v[2:5]\n"
    "s_setprio 0\n"
    ".Lk34:\n"
    "s_waitcnt lgkmcnt(0)\n"
    "s_barrier\n"
    "s_cmp_ge_u32 s44, 8\n"
    "s_cbranch_scc1 .Lk35\n"
    "ds_read2st64_b32 v[2:3], v150 offset0:0 offset1:10\n"
    "ds_read2st64_b32 v[4:5], v150 offset0:20 offset1:30\n"
    "s_waitcnt vmcnt(2)\n"
    "ds_read2st64_b32 v[6:7], v150 offset0:40 offset1:50\n"
    "s_waitcnt vmcnt(0)\n"
    "ds_read2st64_b32 v[8:9], v150 offset0:60 offset1:70\n"
    "ds_read_b32 v86, v150 offset:20480\n"
    "s_waitcnt lgkmcnt(3)\n"
    "v_mfma_f32_32x32x16_bf16 v[58:73], v[38:41], v[2:5], 0\n"
    "s_waitcnt lgkmcnt(0)\n"
    "v_mfma_f32_32x32x16_bf16 v[58:73], v[74:77], v[6:9], v[58:73]\n"
    "v_mfma_f32_32x32x16_bf16 v[58:73], v[34:37], v[86:89], v[58:73]\n"
    "v_mfma_f32_32x32x16_bf16 v[10:25], v[78:81], v[2:5], 0\n"
    "v_cvt_pk_bf16_f32 v58, v58, v59\n"
    "v_or_b32 v58, 1, v58\n"
    "v_cvt_pk_bf16_f32 v60, v60, v61\n"
    "v_or_b32 v60, 1, v60\n"
    "v_cvt_pk_bf16_f32 v62, v62, v63\n"
    "v_or_b32 v62, 1, v62\n"
    "v_mfma_f32_32x32x16_bf16 v[10:25], v[82:85], v[6:9], v[10:25]\n"
    "buffer_store_dword v58, v1, s[16:19], s28 offen sc1\n"
    "buffer_store_dword v60, v1, s[16:19], s29 offen sc1\n"
    "buffer_store_dword v62, v1, s[16:19], s30 offen sc1\n"
    "v_cvt_pk_bf16_f32 v64, v64, v65\n"
    "v_or_b32 v64, 1, v64\n"
    "v_cvt_pk_bf16_f32 v66, v66, v67\n"
    "v_mfma_f32_32x32x16_bf16 v[10:25], v[36:39], v[86:89], v[10:25]\n"
    "buffer_store_dword v64, v1, s[16:19], s31 offen sc1\n"
    "v_or_b32 v66, 1, v66\n"
    "v_cvt_pk_bf16_f32 v68, v68, v69\n"
    "v_or_b32 v68, 1, v68\n"
    "v_cvt_pk_bf16_f32 v70, v70, v71\n"
    "v_or_b32 v70, 1, v70\n"
    "v_cvt_pk_bf16_f32 v72, v72, v73\n"
    "v_or_b32 v72, 1, v72\n"
    "v_cvt_pk_bf16_f32 v10, v10, v11\n"
    "v_or_b32 v10, 1, v10\n"
    "buffer_store_dword v66, v1, s[16:19], s32 offen sc1\n"
    "buffer_store_dword v68, v1, s[16:19], s33 offen sc1\n"
    "buffer_store_dword v70, v1, s[16:19], s34 offen sc1\n"
    "buffer_store_dword v72, v1, s[16:19], s35 offen sc1\n"
    "buffer_store_dword v10, v1, s[16:19], s36 offen sc1\n"
    "s_branch .Lk16\n"
    ".Lk35:\n"
    "s_cmp_eq_u32 s44, 11\n"
    "s_cbranch_scc0 .Lk16\n"
    "v_mov_b32 v163, s53\n"
    "s_cmp_eq_u32 s50, 0\n"
    "s_cbranch_scc0 .Lk16\n"
    "s_mov_b64 exec, 1\n"
    "buffer_store_dword v163, off, s[16:19], 0 sc1\n"
    "s_branch .Lk16\n"
    ".Lk17:\n"
    "s_cmp_ge_u32 s44, 9\n"
    "s_cbranch_scc1 .Lk16\n"
    "s_sleep 110\n"
    ".Lk16:\n"
    "s_cmp_ge_u32 s44, 9\n"
    "s_cbranch_scc0 .Lk36\n"
    "s_endpgm\n"
    ".Lk36:\n"
    "s_mul_i32 s55, s44, 2560\n"
    "s_add_u32 s55, s55, 52736\n"
    "s_mul_i32 s28, s48, 2304\n"
    "s_lshl_b32 s56, s49, 6\n"
    "s_add_u32 s28, s28, s56\n"
    "s_mul_i32 s56, s44, 36864\n"
    "s_add_u32 s28, s28, s56\n"
    "s_add_u32 s28, s28, 1152\n"
    "s_add_u32 s29, s28, 589824\n"
    "v_fma_f32 v157, v158, 2.0, v146\n"
    "v_fma_f32 v153, v158, 4.0, v146\n"
    "v_fmamk_f32 v144, v158, 0x40c00000, v146\n"
    "s_add_u32 s30, s28, 1179648\n"
    "v_cos_f32 v160, v146\n"
    "v_sin_f32 v159, v146\n"
    "v_cos_f32 v156, v157\n"
    "v_sin_f32 v154, v157\n"
    "s_waitcnt vmcnt(0)\n"
    "v_cos_f32 v148, v153\n"
    "v_sin_f32 v145, v153\n"
    "v_cos_f32 v143, v144\n"
    "v_sin_f32 v142, v144\n"
    "s_add_u32 s31, s28, 1769472\n"
    "v_lshlrev_b32 v161, 15, v165\n"
    "v_and_b32 v162, 31, v0\n"
    "s_add_u32 s32, s28, 2359296\n"
    "v_xor_b32 v26, v26, v151\n"
    "v_xor_b32 v27, v27, v151\n"
    "v_xor_b32 v28, v28, v151\n"
    "v_xor_b32 v29, v29, v151\n"
    "v_bitop3_b32 v30, v30, v151, v161 bitop3:0x96\n"
    "v_mul_u32_u24 v162, 80, v162\n"
    "v_mul_u32_u24 v1, 294912, v165\n"
    "s_add_u32 s33, s28, 2949120\n"
    "v_cvt_pk_bf16_f32 v34, v160, v159\n"
    "v_cvt_pk_bf16_f32 v35, v156, v154\n"
    "v_cvt_pk_bf16_f32 v36, v148, v145\n"
    "v_cvt_pk_bf16_f32 v37, v143, v142\n"
    "v_xor_b32 v151, 32768, v151\n"
    "v_lshl_add_u32 v162, v165, 4, v162\n"
    "v_mul_u32_u24 v163, 18432, v164\n"
    "v_lshl_add_u32 v1, v167, 2, v1\n"
    "s_add_u32 s34, s28, 3538944\n"
    "v_xor_b32 v38, v34, v152\n"
    "v_xor_b32 v39, v35, v152\n"
    "v_xor_b32 v40, v36, v152\n"
    "v_xor_b32 v41, v37, v152\n"
    "v_mov_b32_e64 v42, v147\n"
    "v_add_u32 v43, 1024, v147\n"
    "v_add_u32 v44, 1280, v147\n"
    "v_add_u32 v45, 1536, v147\n"
    "v_add_u32 v46, 1792, v147\n"
    "v_xor_b32 v31, v31, v151\n"
    "v_xor_b32 v32, v32, v151\n"
    "v_xor_b32 v33, v33, v151\n"
    "v_add_u32 v162, s55, v162\n"
    "v_add_u32 v1, v1, v163\n"
    "s_add_u32 s35, s28, 4128768\n"
    "s_and_b32 s55, s44, 7\n"
    "s_cbranch_scc1 .Lk37\n"
    "v_and_b32 v42, 4294967231, v147\n"
    "s_lshl_b32 s55, s44, 4\n"
    "s_sub_u32 s55, 2112, s55\n"
    "s_cmp_eq_u32 s44, 0\n"
    "v_lshlrev_b32 v163, 7, v165\n"
    "v_lshl_add_u32 v146, v167, 2, s55\n"
    "v_sub_u32_e64 v146, v146, v163\n"
    "v_subrev_u32 v43, 1024, v146\n"
    "v_subrev_u32 v44, 1280, v146\n"
    "v_subrev_u32 v45, 1536, v146\n"
    "v_subrev_u32 v46, 1792, v146\n"
    "v_xor_b32 v39, 32768, v39\n"
    "v_xor_b32 v40, 32768, v40\n"
    "v_xor_b32 v41, 32768, v41\n"
    "s_cselect_b32 exec_lo, 0, -1\n"
    "v_xor_b32 v38, 32768, v38\n"
    "s_not_b64 exec, exec\n"
    "v_subrev_u32 v43, 64, v43\n"
    "s_mov_b64 exec, -1\n"
    ".Lk37:\n"
    "s_cmp_lt_i32 s50, 0\n"
    "s_cselect_b32 s56, 8, s44\n"
    "s_waitcnt vmcnt(0)\n"
    "s_barrier\n"
    "s_mov_b32 s57, 32768\n"
    ".Lk38:\n"
    "buffer_load_dword v2, v42, s[16:19], s52 offen offset:0 sc1\n"
    "buffer_load_dword v3, v42, s[16:19], s52 offen offset:256 sc1\n"
    "buffer_load_dword v4, v42, s[16:19], s52 offen offset:512 sc1\n"
    "buffer_load_dword v5, v42, s[16:19], s52 offen offset:768 sc1\n"
    "buffer_load_dword v6, v43, s[16:19], s52 offen sc1\n"
    "buffer_load_dword v7, v44, s[16:19], s52 offen sc1\n"
    "buffer_load_dword v8, v45, s[16:19], s52 offen sc1\n"
    "buffer_load_dword v9, v46, s[16:19], s52 offen sc1\n"
    "s_waitcnt vmcnt(5)\n"
    "v_bitop3_b32 v163, v2, v3, v4 bitop3:0x80\n"
    "s_waitcnt vmcnt(3)\n"
    "v_bitop3_b32 v163, v163, v5, v6 bitop3:0x80\n"
    "s_waitcnt vmcnt(1)\n"
    "v_bitop3_b32 v163, v163, v7, v8 bitop3:0x80\n"
    "s_waitcnt vmcnt(0)\n"
    "v_bitop3_b32 v163, v163, v9, 1 bitop3:0x80\n"
    "v_cmp_eq_u32 vcc, 0, v163\n"
    "s_cbranch_vccz .Lk39\n"
    "s_cmp_lt_u32 s56, 8\n"
    "s_cbranch_scc1 .Lk40\n"
    "s_sleep 4\n"
    ".Lk40:\n"
    "s_sub_u32 s57, s57, 1\n"
    "s_cbranch_scc0 .Lk38\n"
    ".Lk39:\n"
    "v_mfma_f32_32x32x16_bf16 v[58:73], v[34:37], v[2:5], 0\n"
    "v_mfma_f32_32x32x16_bf16 v[58:73], v[38:41], v[6:9], v[58:73]\n"
    "s_nop 5\n"
    "v_cvt_pk_bf16_f32 v58, v58, v59\n"
    "v_cvt_pk_bf16_f32 v60, v60, v61\n"
    "v_cvt_pk_bf16_f32 v62, v62, v63\n"
    "v_cvt_pk_bf16_f32 v64, v64, v65\n"
    "v_cvt_pk_bf16_f32 v66, v66, v67\n"
    "v_cvt_pk_bf16_f32 v68, v68, v69\n"
    "v_cvt_pk_bf16_f32 v70, v70, v71\n"
    "v_cvt_pk_bf16_f32 v72, v72, v73\n"
    "ds_write2st64_b32 v149, v58, v60 offset0:0 offset1:10\n"
    "ds_write2st64_b32 v149, v62, v64 offset0:20 offset1:30\n"
    "ds_write2st64_b32 v149, v66, v68 offset0:40 offset1:50\n"
    "ds_write2st64_b32 v149, v70, v72 offset0:60 offset1:70\n"
    "s_waitcnt lgkmcnt(0)\n"
    "s_barrier\n"
    "s_cmp_ge_u32 s44, 8\n"
    "s_cbranch_scc1 .Lk41\n"
    "ds_read_b128 v[2:5], v162\n"
    "ds_read_b128 v[6:9], v162 offset:32\n"
    "s_waitcnt lgkmcnt(1)\n"
    "v_mfma_f32_32x32x16_bf16 v[58:73], v[26:29], v[2:5], 0\n"
    "s_waitcnt lgkmcnt(0)\n"
    "v_mfma_f32_32x32x16_bf16 v[58:73], v[30:33], v[6:9], v[58:73]\n"
    "s_nop 0\n"
    "s_nop 4\n"
    "v_cvt_pk_bf16_f32 v58, v59, v58\n"
    "v_cvt_pk_bf16_f32 v60, v61, v60\n"
    "v_cvt_pk_bf16_f32 v62, v63, v62\n"
    "v_cvt_pk_bf16_f32 v64, v65, v64\n"
    "v_cvt_pk_bf16_f32 v66, v67, v66\n"
    "v_cvt_pk_bf16_f32 v68, v69, v68\n"
    "v_cvt_pk_bf16_f32 v70, v71, v70\n"
    "v_cvt_pk_bf16_f32 v72, v73, v72\n"
    "buffer_store_dword v58, v1, s[20:23], s28 offen sc1\n"
    "buffer_store_dword v60, v1, s[20:23], s29 offen sc1\n"
    "buffer_store_dword v62, v1, s[20:23], s30 offen sc1\n"
    "buffer_store_dword v64, v1, s[20:23], s31 offen sc1\n"
    "buffer_store_dword v66, v1, s[20:23], s32 offen sc1\n"
    "buffer_store_dword v68, v1, s[20:23], s33 offen sc1\n"
    "buffer_store_dword v70, v1, s[20:23], s34 offen sc1\n"
    "buffer_store_dword v72, v1, s[20:23], s35 offen sc1\n"
    ".Lk41:\n"
    "s_endpgm\n"
    ".Lklast:\n"
    "s_endpgm\n"
    :
    : "s"(__builtin_amdgcn_kernarg_segment_ptr()), "s"(__builtin_amdgcn_workgroup_id_x()), "v"(tid),
      "s"((u32)(size_t)(__attribute__((address_space(3))) char*)smem)
    : "v1", "v2", "v3", "v4", "v5", "v6", "v7", "v8", "v9", "v10", "v11", "v12", "v13", "v14", "v15", "v16", "v17", "v18", "v19", "v20", "v21", "v22", "v23", "v24", "v25", "v26", "v27", "v28", "v29", "v30", "v31", "v32", "v33", "v34", "v35", "v36", "v37", "v38", "v39", "v40", "v41", "v42", "v43", "v44", "v45", "v46", "v47", "v48", "v49", "v50", "v51", "v52", "v53", "v54", "v55", "v56", "v57", "v58", "v59", "v60", "v61", "v62", "v63", "v64", "v65", "v66", "v67", "v68", "v69", "v70", "v71", "v72", "v73", "v74", "v75", "v76", "v77", "v78", "v79", "v80", "v81", "v82", "v83", "v84", "v85", "v86", "v87", "v88", "v89", "v90", "v91", "v92", "v93", "v94", "v95", "v96", "v97", "v98", "v99", "v100", "v101", "v102", "v103", "v104", "v105", "v106", "v107", "v108", "v109", "v110", "v111", "v112", "v113", "v114", "v115", "v116", "v117", "v118", "v119", "v120", "v121", "v122", "v123", "v124", "v125", "v126", "v127", "v128", "v129", "v130", "v131", "v132", "v133", "v134", "v135", "v136", "v137", "v138", "v139", "v140", "v141", "v142", "v143", "v144", "v145", "v146", "v147", "v148", "v149", "v150", "v151", "v152", "v153", "v154", "v155", "v156", "v157", "v158", "v159", "v160", "v161", "v162", "v163", "v164", "v165", "v166", "v167", "s4", "s5", "s6", "s7", "s8", "s9", "s10", "s11", "s12", "s13", "s14", "s15", "s16", "s17", "s18", "s19", "s20", "s21", "s22", "s23", "s24", "s25", "s26", "s27", "s28", "s29", "s30", "s31", "s33", "s34", "s35", "s36", "s37", "s38", "s39", "s40", "s41", "s42", "s43", "s44", "s45", "s46", "s47", "s48", "s49", "s50", "s51", "s52", "s53", "s54", "s55", "s56", "s57", "s58", "s59", "s60", "s61", "s62", "s63", "s64", "s65", "s66", "s67", "vcc", "scc", "memory");
#endif
}

extern "C" void kernel_launch(void* const* d_in, const int* in_sizes, int n_in, void* d_out, int out_size, void* d_ws, size_t ws_size,
                              hipStream_t stream) {
  const u32 F = 0x00020000u;
  fused_kernel<<<256, 768, 0, stream>>>(d_in[9], 2048u * 1152u, F, d_in[10], 2048u * 578u, F, d_in[11], 2048u * 578u, F,
                                        d_ws, 16u << 20, F, d_out, 2048u * 2304u, F, d_in[0], 2048u * 1152u, F,
                                        d_in[1], d_in[2], d_in[3], d_in[4], d_in[5], d_in[6], d_in[7], d_in[8]);
}
```
